```python
import torch

def _softmax(x, axis=-1):
    return torch.softmax(x, dim=axis)
def _gelu(x):
    return torch.nn.functional.gelu(x, approximate="tanh")
def _silu(x):
    return torch.nn.functional.silu(x)
def _sigmoid(x):
    return torch.sigmoid(x)
def _layernorm(x, axis=-1, eps=1e-5):
    m = x.mean(dim=axis, keepdim=True)
    v = x.var(dim=axis, keepdim=True, unbiased=False)
    return (x - m) / torch.sqrt(v + eps)
def _scatter_add(tgt, idx, src, axis=0):
    return tgt.clone().index_add_(axis, idx, src)
def _segment_sum(src, seg, n):
    out = torch.zeros((n,) + src.shape[1:], dtype=src.dtype)
    return out.index_add_(0, seg, src)
def _pad(x, pw):
    flat = tuple(v for pair in reversed(pw) for v in pair)
    return torch.nn.functional.pad(x, flat)

kernel_name = 'concat_heads__gqa_block_cached__sliding_block_cached__decode'

def setup_inputs():

    _w = (lambda a: (lambda t: t.to(torch.bfloat16) if t.is_floating_point() else t)(torch.as_tensor(a)))
    def _A_setup():
        B, Sq, D = 2, 1, 512
        Hq, Hkv, Sk, Dh = 32, 8, 8192, 128
        assert Hq % Hkv == 0, (Hq, Hkv)
        return {
            "x": _w((lambda *s: torch.randn(*s, dtype=torch.bfloat16) * 0.8)(B, Sq, D)),
            "k_cache": _w((lambda *s: torch.randn(*s, dtype=torch.bfloat16) * 1.6)(B, Hkv, Sk, Dh)),
            "v_cache": _w((lambda *s: torch.randn(*s, dtype=torch.bfloat16) * 0.2)(B, Hkv, Sk, Dh)),
            "wq": _w((lambda *s: torch.randn(*s, dtype=torch.bfloat16) * 1.6)(D, Hq * Dh) * (D ** -0.5)),
            "wo": _w((lambda *s: torch.randn(*s, dtype=torch.bfloat16) * 0.2)(Hq * Dh, D) * ((Hq * Dh) ** -0.5)),
        }
    _A_in = _A_setup()

    def _B_setup():
        B, Sq, D = 2, 1, 512
        Hq, Hkv, Sk, Dh = 32, 8, 8192, 128
        assert Hq % Hkv == 0, (Hq, Hkv)
        return {
            "x": _w((lambda *s: torch.randn(*s, dtype=torch.bfloat16) * 0.8)(B, Sq, D)),
            "k_cache": _w((lambda *s: torch.randn(*s, dtype=torch.bfloat16) * 1.6)(B, Hkv, Sk, Dh)),
            "v_cache": _w((lambda *s: torch.randn(*s, dtype=torch.bfloat16) * 0.2)(B, Hkv, Sk, Dh)),
            "wq": _w((lambda *s: torch.randn(*s, dtype=torch.bfloat16) * 1.6)(D, Hq * Dh) * (D ** -0.5)),
            "wo": _w((lambda *s: torch.randn(*s, dtype=torch.bfloat16) * 0.2)(Hq * Dh, D) * ((Hq * Dh) ** -0.5)),
        }
    _B_in = _B_setup()

    return {
        "A_x": _A_in["x"],
        "A_k_cache": _A_in["k_cache"],
        "A_v_cache": _A_in["v_cache"],
        "A_wq": _A_in["wq"],
        "A_wo": _A_in["wo"],
        "B_x": _B_in["x"],
        "B_k_cache": _B_in["k_cache"],
        "B_v_cache": _B_in["v_cache"],
        "B_wq": _B_in["wq"],
        "B_wo": _B_in["wo"],
    }

def reference(A_x, A_k_cache, A_v_cache, A_wq, A_wo, B_x, B_k_cache, B_v_cache, B_wq, B_wo):
    def _A_ref(x, k_cache, v_cache, wq, wo):
        B, Sq, D = x.shape
        Hq, Dh = 32, 128
        Hkv = k_cache.shape[1]
        rep = Hq // Hkv
        q = (lambda x, *d: x.permute(*d))((x @ wq).reshape(B, Sq, Hq, Dh), 0, 2, 1, 3)
        k = (lambda x, n, axis: torch.repeat_interleave(x, n, dim=axis))(k_cache, rep, axis=1)
        v = (lambda x, n, axis: torch.repeat_interleave(x, n, dim=axis))(v_cache, rep, axis=1)
        scores = torch.einsum("bhqd,bhkd->bhqk", q, k) * (Dh ** -0.5)
        p = _softmax(scores, axis=-1)
        o = torch.einsum("bhqk,bhkd->bhqd", p, v)
        return (lambda x, *d: x.permute(*d))(o, 0, 2, 1, 3).reshape(B, Sq, Hq * Dh) @ wo

    def _B_ref(x, k_cache, v_cache, wq, wo):
        B, Sq, D = x.shape
        Hq, Dh = 32, 128
        Hkv, Sk = k_cache.shape[1], k_cache.shape[2]
        W = min(4096, max(256, Sk // 4))
        rep = Hq // Hkv
        q = (lambda x, *d: x.permute(*d))((x @ wq).reshape(B, Sq, Hq, Dh), 0, 2, 1, 3)
        k = (lambda x, n, axis: torch.repeat_interleave(x, n, dim=axis))(k_cache[:, :, Sk - W:, :], rep, axis=1)
        v = (lambda x, n, axis: torch.repeat_interleave(x, n, dim=axis))(v_cache[:, :, Sk - W:, :], rep, axis=1)
        scores = torch.einsum("bhqd,bhkd->bhqk", q, k) * (Dh ** -0.5)
        p = _softmax(scores, axis=-1)
        o = torch.einsum("bhqk,bhkd->bhqd", p, v)
        return (lambda x, *d: x.permute(*d))(o, 0, 2, 1, 3).reshape(B, Sq, Hq * Dh) @ wo

    a = _A_ref(A_x, A_k_cache, A_v_cache, A_wq, A_wo)
    b = _B_ref(B_x, B_k_cache, B_v_cache, B_wq, B_wo)
    return torch.concatenate([a, b], axis=-1)
```

```cpp
#include <hip/hip_runtime.h>
#include <hip/hip_bf16.h>
#include <stdint.h>

typedef unsigned short u16;
typedef unsigned u32x4 __attribute__((ext_vector_type(4)));
typedef float f32x4 __attribute__((ext_vector_type(4)));
typedef short bf16x8 __attribute__((ext_vector_type(8)));

__device__ __forceinline__ float bf2f(u16 v) { return __uint_as_float(((unsigned)v) << 16); }
__device__ __forceinline__ u16 f2bf(float f) { __hip_bfloat16 h = __float2bfloat16(f); return *(u16*)&h; }

struct Ptrs { const u16* x[2]; const u16* k[2]; const u16* v[2]; const u16* wq[2]; const u16* wo[2]; };

__global__ void __launch_bounds__(256) k_q(Ptrs p, u16* q) {
  int br = blockIdx.x >> 6, cg = blockIdx.x & 63;
  int t = threadIdx.x, c = t & 63, rq = t >> 6;
  const u16* wq = p.wq[br]; const u16* x = p.x[br];
  float a0 = 0, a1 = 0;
  for (int r = rq * 128; r < rq * 128 + 128; ++r) {
    float w = bf2f(wq[(size_t)r * 4096 + cg * 64 + c]);
    a0 += bf2f(x[r]) * w; a1 += bf2f(x[512 + r]) * w;
  }
  __shared__ float s[2][4][64];
  s[0][rq][c] = a0; s[1][rq][c] = a1;
  __syncthreads();
  if (t < 128) { int b = t >> 6; float v = s[b][0][c] + s[b][1][c] + s[b][2][c] + s[b][3][c];
    q[(br * 2 + b) * 4096 + cg * 64 + c] = f2bf(v); }
}

__global__ void __launch_bounds__(256) k_attn(Ptrs p, const u16* q, float* pm, float* pl, float* pacc) {
  int blk = blockIdx.x; int br, bk, ch;
  if (blk < 1024) { br = 0; bk = blk >> 6; ch = blk & 63; } else { br = 1; bk = (blk - 1024) >> 4; ch = (blk - 1024) & 15; }
  int b = bk >> 3, kvh = bk & 7;
  int key0 = (br == 0 ? 0 : 6144) + ch * 128;
  const u16* K = p.k[br] + ((size_t)bk * 8192 + key0) * 128;
  const u16* V = p.v[br] + ((size_t)bk * 8192 + key0) * 128;
  __shared__ float sq[4][128]; __shared__ float ss[4][128]; __shared__ float sm[4], sl[4];
  int t = threadIdx.x;
  for (int i = t; i < 512; i += 256) sq[i >> 7][i & 127] = bf2f(q[(br * 2 + b) * 4096 + kvh * 512 + i]);
  __syncthreads();
  { int key = t >> 1, hp = t & 1; float a0 = 0, a1 = 0;
    for (int d = 0; d < 128; ++d) { float kv = bf2f(K[key * 128 + d]); a0 += kv * sq[hp * 2][d]; a1 += kv * sq[hp * 2 + 1][d]; }
    ss[hp * 2][key] = a0 * 0.08838834764831845f; ss[hp * 2 + 1][key] = a1 * 0.08838834764831845f; }
  __syncthreads();
  if (t < 4) { float m = -INFINITY; for (int i = 0; i < 128; ++i) m = fmaxf(m, ss[t][i]);
    float l = 0; for (int i = 0; i < 128; ++i) { float e = __expf(ss[t][i] - m); ss[t][i] = e; l += e; } sm[t] = m; sl[t] = l; }
  __syncthreads();
  { int d = t & 127, hp = t >> 7; float a0 = 0, a1 = 0;
    for (int key = 0; key < 128; ++key) { float vv = bf2f(V[key * 128 + d]); a0 += ss[hp * 2][key] * vv; a1 += ss[hp * 2 + 1][key] * vv; }
    pacc[((size_t)blk * 4 + hp * 2) * 128 + d] = a0; pacc[((size_t)blk * 4 + hp * 2 + 1) * 128 + d] = a1; }
  if (t < 4) { pm[blk * 4 + t] = sm[t]; pl[blk * 4 + t] = sl[t]; }
}

__global__ void __launch_bounds__(128) k_comb(const float* pm, const float* pl, const float* pacc, float* o) {
  int id = blockIdx.x; int br = id >> 6, b = (id >> 5) & 1, h = id & 31; int kvh = h >> 2, hh = h & 3;
  int bk = b * 8 + kvh; int n = br == 0 ? 64 : 16; int base = br == 0 ? bk * 64 : 1024 + bk * 16;
  float M = -INFINITY; for (int i = 0; i < n; ++i) M = fmaxf(M, pm[(base + i) * 4 + hh]);
  float L = 0, a = 0;
  for (int i = 0; i < n; ++i) { float w = __expf(pm[(base + i) * 4 + hh] - M); L += w * pl[(base + i) * 4 + hh]; a += w * pacc[((size_t)(base + i) * 4 + hh) * 128 + threadIdx.x]; }
  o[(br * 2 + b) * 4096 + h * 128 + threadIdx.x] = bf2f(f2bf(a / L));
}

__global__ void __launch_bounds__(256) k_o(Ptrs p, const float* o, u16* out) {
  int br = blockIdx.x >> 5, cg = blockIdx.x & 31; int t = threadIdx.x, c = t & 15, rg = t >> 4;
  const u16* wo = p.wo[br]; float a0 = 0, a1 = 0;
  for (int r = rg * 256; r < rg * 256 + 256; ++r) { float w = bf2f(wo[(size_t)r * 512 + cg * 16 + c]); a0 += o[(br * 2) * 4096 + r] * w; a1 += o[(br * 2 + 1) * 4096 + r] * w; }
  __shared__ float s[2][16][16];
  s[0][rg][c] = a0; s[1][rg][c] = a1; __syncthreads();
  if (t < 32) { int b = t >> 4; float v = 0; for (int i = 0; i < 16; ++i) v += s[b][i][c]; out[b * 1024 + br * 512 + cg * 16 + c] = f2bf(v); }
}

extern "C" void kernel_launch(void* const* d_in, const int* in_sizes, int n_in, void* d_out, int out_size, void* d_ws, size_t ws_size, hipStream_t stream) {
  Ptrs p;
  for (int br = 0; br < 2; ++br) { p.x[br] = (const u16*)d_in[br * 5 + 0]; p.k[br] = (const u16*)d_in[br * 5 + 1]; p.v[br] = (const u16*)d_in[br * 5 + 2]; p.wq[br] = (const u16*)d_in[br * 5 + 3]; p.wo[br] = (const u16*)d_in[br * 5 + 4]; }
  char* ws = (char*)d_ws;
  u16* q = (u16*)ws; float* pm = (float*)(ws + 65536); float* pl = (float*)(ws + 131072); float* o = (float*)(ws + 196608); float* pacc = (float*)(ws + 1048576);
  k_q<<<128, 256, 0, stream>>>(p, q);
  k_attn<<<1280, 256, 0, stream>>>(p, q, pm, pl, pacc);
  k_comb<<<128, 128, 0, stream>>>(pm, pl, pacc, o);
  k_o<<<64, 256, 0, stream>>>(p, o, (u16*)d_out);
}
```

```cpp
#include <hip/hip_runtime.h>
#include <stdint.h>

typedef unsigned short u16;
typedef unsigned int u32;
typedef unsigned long long u64;
typedef u32 u32x2 __attribute__((ext_vector_type(2)));
typedef u32 u32x4 __attribute__((ext_vector_type(4)));
typedef float f32x2 __attribute__((ext_vector_type(2)));
typedef float f32x4 __attribute__((ext_vector_type(4)));
typedef short s16x8 __attribute__((ext_vector_type(8)));
typedef __bf16 bf16x2 __attribute__((ext_vector_type(2)));
#define GAS __attribute__((address_space(1)))
#define LAS __attribute__((address_space(3)))
#define CAS __attribute__((address_space(4)))

#define WIN 2
#define VPRE 4
#define WINV 3
#define WINQ 16
#define WINO 6
#define ABL 0
#define RDY_DELAY 4
#define H1_SLEEP2 0
#define H1_MANY 32
#define H1_LINEMASK 1
#define H1_SLEEP 2
#define KV_NT 1
#define W_NT 1
#define POLL_SLEEP 16

struct Params {
  const u16* x[2]; const u16* k[2]; const u16* v[2]; const u16* wq[2]; const u16* wo[2];
  u16* out; unsigned char* ws;
};

constexpr int WS_E = 0, WS_MS = 1 << 15, WS_MQ = 1 << 16, WS_H2 = 3 << 16, WS_H1 = 1 << 19, WS_BYTES = 2 << 20;
constexpr int H1_STR = 4096 + 128, H2_STR = 1024 + 128;
constexpr u32 SENT = 0xFFFFFFFFu;
constexpr int AUX_SC1 = 16;
constexpr u32 SPIN_CAP = 1u << 20;

#define STAMP(i)
#define STAMP_FLUSH(i)

__device__ __forceinline__ u32x4 ldg_kv(const u16* ptr) {
  return __builtin_nontemporal_load((const GAS u32x4*)ptr);
}
__device__ __forceinline__ u32x4 ldg_w(const u16* ptr) {
  return __builtin_nontemporal_load((const GAS u32x4*)ptr);
}
__device__ __forceinline__ f32x4 mfma16(u32x4 a, u32x4 b, f32x4 c) {
  return __builtin_amdgcn_mfma_f32_16x16x32_bf16(__builtin_bit_cast(s16x8, a), __builtin_bit_cast(s16x8, b), c, 0, 0, 0);
}
__device__ __forceinline__ u32 cvtpk(float lo, float hi) {
  f32x2 v = {lo, hi};
  return __builtin_bit_cast(u32, __builtin_convertvector(v, bf16x2));
}
__device__ __forceinline__ float lo_f(u32 v) { return __builtin_bit_cast(float, v << 16); }
__device__ __forceinline__ float hi_f(u32 v) { return __builtin_bit_cast(float, v & 0xffff0000u); }
__device__ __forceinline__ float dot2(u32 a, u32 b, float acc) {
  return __builtin_amdgcn_fdot2_f32_bf16(__builtin_bit_cast(bf16x2, a), __builtin_bit_cast(bf16x2, b), acc, false);
}
template <int CTRL> __device__ __forceinline__ float dpp(float x) {
  return __builtin_bit_cast(float, __builtin_amdgcn_mov_dpp(__builtin_bit_cast(int, x), CTRL, 0xf, 0xf, true));
}
constexpr int XOR1 = 0xB1, XOR2 = 0x4E, ROR4 = 0x124, ROR8 = 0x128;
__device__ __forceinline__ float s16_max(float x) {
  auto s = __builtin_amdgcn_permlane16_swap(__float_as_uint(x), __float_as_uint(x), false, false);
  return fmaxf(__uint_as_float(s[0]), __uint_as_float(s[1]));
}
__device__ __forceinline__ float s32_max(float x) {
  auto t = __builtin_amdgcn_permlane32_swap(__float_as_uint(x), __float_as_uint(x), false, false);
  return fmaxf(__uint_as_float(t[0]), __uint_as_float(t[1]));
}
__device__ __forceinline__ float s16_sum(float x) {
  auto s = __builtin_amdgcn_permlane16_swap(__float_as_uint(x), __float_as_uint(x), false, false);
  return __uint_as_float(s[0]) + __uint_as_float(s[1]);
}
__device__ __forceinline__ float s32_sum(float x) {
  auto t = __builtin_amdgcn_permlane32_swap(__float_as_uint(x), __float_as_uint(x), false, false);
  return __uint_as_float(t[0]) + __uint_as_float(t[1]);
}
__device__ __forceinline__ u32 umax(u32 a, u32 b) { return a > b ? a : b; }
__device__ __forceinline__ u32 nos(float v) { u32 u = __float_as_uint(v); return u < 0xFFFFFFFEu ? u : 0xFFFFFFFEu; }
__device__ __forceinline__ u32x4 nos4(f32x4 v) { u32x4 r = {nos(v[0]), nos(v[1]), nos(v[2]), nos(v[3])}; return r; }
__device__ __forceinline__ u32 umax4(u32x4 v) { return umax(umax(v[0], v[1]), umax(v[2], v[3])); }
__device__ __forceinline__ u32 btag(float v, u32 tb) { return ((__float_as_uint(v) + 0x80u) & 0xFFFFFF00u) | tb; }
__device__ __forceinline__ u32x4 btag4(f32x4 v, u32 t) { u32x4 r = {btag(v[0], t & 255u), btag(v[1], (t >> 8) & 255u), btag(v[2], (t >> 16) & 255u), btag(v[3], t >> 24)}; return r; }
__device__ __forceinline__ u32x4 untag4(u32x4 v, u32 t) { u32x4 r = {v[0] ^ (t & 255u), v[1] ^ ((t >> 8) & 255u), v[2] ^ ((t >> 16) & 255u), v[3] ^ (t >> 24)}; return r; }
__device__ __forceinline__ bool stale4(u32x4 v) { return ((v[0] | v[1] | v[2] | v[3]) & 255u) != 0; }
__device__ __forceinline__ u64 mix64(u64 z) {
  z += 0x9E3779B97F4A7C15ull; z = (z ^ (z >> 30)) * 0xBF58476D1CE4E5B9ull; z = (z ^ (z >> 27)) * 0x94D049BB133111EBull; return z ^ (z >> 31);
}
#define BAR() do { asm volatile("s_waitcnt lgkmcnt(0)" ::: "memory"); __builtin_amdgcn_s_barrier(); asm volatile("" ::: "memory"); } while (0)
#define VMCNT(n) asm volatile("s_waitcnt vmcnt(%0)" :: "i"(n) : "memory")
#define LGKM0() asm volatile("s_waitcnt lgkmcnt(0)" ::: "memory")
#define CBAR() asm volatile("" ::: "memory")

__device__ __forceinline__ u32x4 tr_frag(const u32x4* w, int j) {
  u32x4 r;
#pragma unroll
  for (int t = 0; t < 4; ++t) r[t] = __builtin_amdgcn_perm(w[2 * t + 1][j >> 1], w[2 * t][j >> 1], (j & 1) ? 0x07060302u : 0x05040100u);
  return r;
}

constexpr int RED_HS = 132;
constexpr int L_WO = 0;
constexpr int L_RED = 32768;
constexpr int L_SQ = L_RED + 21120;
constexpr int L_ST = L_SQ + 2048;
constexpr int L_BETA = L_ST + 640;
constexpr int L_QP = L_BETA + 192;
constexpr int L_O2 = L_QP + 4096;
constexpr int L_WT = L_O2 + 512;
constexpr int L_FLAG = L_WT + 128;
constexpr int L_BYTES = L_FLAG + 48;

__device__ __forceinline__ void lds_wait_flag(LAS unsigned char* L, int off, u64 tag) {
#pragma nounroll
  for (u32 spins = 0; spins < SPIN_CAP; ++spins) {
    if (*(volatile LAS u64*)(L + off) == tag) break;
    __builtin_amdgcn_s_sleep(1);
  }
  CBAR();
}

#define MAKE_TAG() \
  __builtin_amdgcn_sched_barrier(0); \
  u64 E; asm volatile("s_load_dwordx2 %0, %1, 0x0\n\ts_waitcnt lgkmcnt(0)" : "=s"(E) : "s"(p.ws) : "memory"); \
  const u64 mixed = mix64(E); \
  const u32 tx_ = (u32)mixed ^ (u32)E; \
  const u32 tlo = (u32)mixed ^ ((tx_ - 0x01010101u) & ~tx_ & 0x80808080u);     \
  const u32 thi = (u32)(mixed >> 32) == (u32)(E >> 32) ? ~(u32)(mixed >> 32) : (u32)(mixed >> 32); \
  const u64 tag = ((u64)thi << 32) | tlo;

__global__ void __launch_bounds__(768, 3) fused_decode(Params p) {
  __shared__ __attribute__((aligned(16))) unsigned char smem[L_BYTES];
  LAS unsigned char* L = (LAS unsigned char*)smem;
  const int blk = blockIdx.x, tid = threadIdx.x, lane = tid & 63;
  const int wave = __builtin_amdgcn_readfirstlane(tid >> 6);
  const int kvh = blk & 7, jj = blk >> 3;
  const int g = lane >> 4, r = lane & 15;
  const __amdgpu_buffer_rsrc_t rs = __builtin_amdgcn_make_buffer_rsrc(p.ws, 0, WS_BYTES, 0x00020000);
  constexpr float LOG2E = 1.4426950408889634f;
  const int cbr = jj >> 4;

  if (wave >= 2) {
    STAMP(14);
    const int kw = wave - 2, br = kw >= 8 ? 1 : 0;
    const int b = jj >> 4, s = jj & 15;
    const int key0 = br ? 6144 + 128 * s + 64 * (kw - 8) : 512 * s + 64 * kw;
    const size_t base = ((size_t)(b * 8 + kvh) * 8192 + key0) * 128;
    const int rot = (kw + jj) & 3;
    const u16* Kp = p.k[br] + base + (r >> 2) * 128 + (4 * g + (r & 3)) * 8;
    const u16* Vp = p.v[br] + base + lane * 8;
    u32x4 Kf[16], Vf[16];
#pragma unroll
    for (int n = 0; n < 16; ++n) {
      if (n >= WIN) VMCNT(WIN - 1);
      Kf[n] = ldg_kv(Kp + ((n >> 2) ^ rot) * 2048 + (n & 3) * 512);
      __builtin_amdgcn_sched_barrier(0);
    }
    STAMP(17);
#pragma unroll
    for (int n = 0; n < VPRE; ++n) {
      Vf[n] = ldg_kv(Vp + ((n >> 2) ^ rot) * 2048 + (n & 3) * 512);
      __builtin_amdgcn_sched_barrier(0);
    }
    STAMP(5);
    MAKE_TAG();
    lds_wait_flag(L, L_FLAG + 16, tag);
    STAMP(6);
    const u32x4 Qf = *(LAS u32x4*)(L + L_SQ + br * 1024 + (r >> 2) * 256 + (4 * g + (r & 3)) * 16);
#define VLOAD(n) do { __builtin_amdgcn_sched_barrier(0); Vf[n] = ldg_kv(Vp + (((n) >> 2) ^ rot) * 2048 + ((n) & 3) * 512); __builtin_amdgcn_sched_barrier(0); } while (0)
    float sc[16];
    {
      f32x4 d[16];
#pragma unroll
      for (int i = 0; i < 16; ++i) d[i] = mfma16(Kf[i], Qf, f32x4{0.f, 0.f, 0.f, 0.f});
      VLOAD(VPRE);
#pragma unroll
      for (int i = 0; i < 16; ++i) sc[i] = (dpp<0x00>(d[i][0]) + dpp<0x55>(d[i][1])) + (dpp<0xAA>(d[i][2]) + dpp<0xFF>(d[i][3]));
    }
    VLOAD(VPRE + 1);
#pragma unroll
    for (int i = 0; i < 16; i += 2) {
      const u32 a0 = cvtpk(sc[i], sc[i + 1]);
      const u32 c0 = cvtpk(lo_f(a0) * 0.08838834764831845f, hi_f(a0) * 0.08838834764831845f);
      sc[i] = lo_f(c0); sc[i + 1] = hi_f(c0);
    }
    VLOAD(VPRE + 2);
    float mx = sc[0];
#pragma unroll
    for (int i = 1; i < 16; ++i) mx = fmaxf(mx, sc[i]);
    mx = s32_max(s16_max(mx));
    const float M2 = mx * LOG2E;
    float ls = 0.f;
#pragma unroll
    for (int i = 0; i < 16; ++i) { sc[i] = __builtin_amdgcn_exp2f(__builtin_fmaf(sc[i], LOG2E, -M2)); ls += sc[i]; }
    ls = s32_sum(s16_sum(ls));
    if ((lane & 51) == 0) *(LAS u32x4*)(L + L_ST + (kw * 4 + (lane >> 2)) * 16) = u32x4{__float_as_uint(M2), tlo, __float_as_uint(ls), thi};
    VLOAD(VPRE + 3);
    u32x4 Pf[2];
#pragma unroll
    for (int u = 0; u < 2; ++u)
#pragma unroll
      for (int t = 0; t < 4; ++t) Pf[u][t] = cvtpk(sc[8 * u + 2 * t], sc[8 * u + 2 * t + 1]);
    STAMP(7);
#pragma unroll
    for (int n = VPRE + 4; n < 16; ++n) { VMCNT(WINV - 1); VLOAD(n); }
    STAMP(9);
    f32x4 acc[8];
#pragma unroll
    for (int j = 0; j < 8; ++j) acc[j] = mfma16(tr_frag(Vf, j), Pf[0], f32x4{0.f, 0.f, 0.f, 0.f});
#pragma unroll
    for (int j = 0; j < 8; ++j) acc[j] = mfma16(tr_frag(Vf + 8, j), Pf[1], acc[j]);
    if ((r & 3) == 0) {
#pragma unroll
      for (int j = 0; j < 8; ++j) *(LAS f32x4*)(L + L_RED + ((kw * 4 + (r >> 2)) * RED_HS + g * 32 + 4 * j) * 4) = acc[j];
    }
    STAMP(8);
    BAR();
    STAMP(10);
    if (kw >= 4) {
      if (kw < 6) {
        const int t = (kw - 4) * 64 + lane;
        const int off = L_RED + ((t >> 5) * RED_HS + (t & 31) * 4) * 4;
        const f32x4 b0 = *(LAS f32x4*)(L + L_BETA + (t >> 5) * 48), b1 = *(LAS f32x4*)(L + L_BETA + (t >> 5) * 48 + 16);
        f32x4 v = *(LAS f32x4*)(L + off) * b0[0];
#pragma unroll
        for (int w = 1; w < 8; ++w) v += *(LAS f32x4*)(L + off + w * 4 * RED_HS * 4) * (w < 4 ? b0[w & 3] : b1[w & 3]);
        __builtin_amdgcn_raw_buffer_store_b128(btag4(v, tlo), rs, WS_H1 + blk * H1_STR + t * 16, 0, AUX_SC1);
      } else if (kw < 8) {
        const int t = (kw - 6) * 64 + lane;
        const int off = L_RED + ((32 + (t >> 5)) * RED_HS + (t & 31) * 4) * 4;
        const f32x2 b0 = *(LAS f32x2*)(L + L_BETA + (t >> 5) * 48 + 32);
        const f32x4 v = *(LAS f32x4*)(L + off) * b0[0] + *(LAS f32x4*)(L + off + 4 * RED_HS * 4) * b0[1];
        __builtin_amdgcn_raw_buffer_store_b128(btag4(v, tlo), rs, WS_H1 + blk * H1_STR + 2048 + t * 16, 0, AUX_SC1);
      }
      return;
    }
    u32x4 wf[8];
    {
      const int hh = (jj >> 2) & 3;
      const int o0 = WS_H1 + (kvh + 8 * (lane >> 3)) * H1_STR + (cbr * 512 + hh * 128 + kw * 32) * 4 + (lane & 7) * 16;
#pragma unroll
      for (int i = 0; i < 8; ++i) { wf[i] = *(LAS u32x4*)(L + L_WO + (((i >> 1) * 8 + 2 * kw + (i & 1)) * 64 + lane) * 16); asm volatile("" : "+v"(wf[i])); }
      u32x4 a[4];
#pragma unroll
      for (int i = 0; i < 4; ++i) a[i] = u32x4{0u, 0u, 0u, 0u};
      u32 issued = 0;
#pragma nounroll
      for (u32 spins = 0; spins < SPIN_CAP; ++spins) {
        const u32x2 rd = *(volatile LAS u32x2*)(L + L_FLAG + 32);
        const u32 todo = __builtin_amdgcn_readfirstlane(rd[1] == thi ? rd[0] : 0u) & ~issued;
        if (todo) {
          const u32 mine = todo >> (lane >> 3);
#pragma unroll
          for (int i = 0; i < 4; ++i) if ((mine >> (8 * i)) & 1u) a[i] = __builtin_amdgcn_raw_buffer_load_b128(rs, o0 + i * 64 * H1_STR, 0, AUX_SC1);
          issued |= todo;
          if (issued == 0xFFFFFFFFu) break;
        }
      }
      u32x4 wu;
#pragma nounroll
      for (u32 spins = 0; spins < SPIN_CAP; ++spins) {
        wu = untag4(*(volatile LAS u32x4*)(L + L_WT + (lane >> 3) * 16), tlo);
        if (!__any(stale4(wu))) break;
      }
      __builtin_amdgcn_sched_barrier(0);
#pragma nounroll
      for (u32 spins = 0; spins < SPIN_CAP; ++spins) {
        u64 miss[4];
#pragma unroll
        for (int i = 0; i < 4; ++i) miss[i] = __ballot(stale4(untag4(a[i], tlo)));
        if ((miss[0] | miss[1] | miss[2] | miss[3]) == 0) break;
        CBAR();
#pragma unroll
        for (int i = 0; i < 4; ++i)
          if ((miss[i] >> (lane & 56)) & 255u) a[i] = __builtin_amdgcn_raw_buffer_load_b128(rs, o0 + i * 64 * H1_STR, 0, AUX_SC1);
      }
      STAMP(11);
      const f32x4 wt = __builtin_bit_cast(f32x4, wu);
      const f32x4 v0 = __builtin_bit_cast(f32x4, untag4(a[0], tlo)) * wt[0] + __builtin_bit_cast(f32x4, untag4(a[1], tlo)) * wt[1];
      const f32x4 v1 = __builtin_bit_cast(f32x4, untag4(a[2], tlo)) * wt[2] + __builtin_bit_cast(f32x4, untag4(a[3], tlo)) * wt[3];
      float x[4];
#pragma unroll
      for (int e = 0; e < 4; ++e) {
        auto t = __builtin_amdgcn_permlane32_swap(__float_as_uint(v0[e]), __float_as_uint(v1[e]), false, false);
        x[e] = __uint_as_float(t[0]) + __uint_as_float(t[1]);
      }
      float y[2];
#pragma unroll
      for (int e = 0; e < 2; ++e) {
        auto t = __builtin_amdgcn_permlane16_swap(__float_as_uint(x[e]), __float_as_uint(x[e + 2]), false, false);
        y[e] = __uint_as_float(t[0]) + __uint_as_float(t[1]);
        y[e] += dpp<ROR8>(y[e]);
      }
      if ((lane & 8) == 0) *(LAS u32*)(L + L_O2 + (lane >> 5) * 256 + kw * 64 + (lane & 7) * 8 + ((lane >> 4) & 1) * 4) = cvtpk(y[0], y[1]);
    }
    BAR();
    STAMP(19);
    {
      f32x4 d0 = {0.f, 0.f, 0.f, 0.f}, d1 = {0.f, 0.f, 0.f, 0.f};
#pragma unroll
      for (int qd = 0; qd < 4; ++qd) {
        const u32x4 Of = *(LAS u32x4*)(L + L_O2 + ((r >> 2) & 1) * 256 + qd * 64 + g * 16);
        d0 = mfma16(Of, wf[2 * qd], d0);
        d1 = mfma16(Of, wf[2 * qd + 1], d1);
      }
      const u32 tsh = tlo >> ((r & 1) * 16);
      if (lane < 32) __builtin_amdgcn_raw_buffer_store_b64(u32x2{btag(d0[0], tsh & 255u), btag(d1[0], (tsh >> 8) & 255u)}, rs, WS_H2 + blk * H2_STR + g * 512 + kw * 128 + r * 8, 0, AUX_SC1);
    }
    return;
  }

  STAMP(0);
  if (!(ABL & 8)) __builtin_amdgcn_s_setprio(3);
  const int hw = wave;
    const int kh = jj & 1, cg = (jj >> 1) & 7;
    const int rr = lane >> 3, c = lane & 7;
    const u16* xp = p.x[cbr] + 256 * kh + 128 * hw + 16 * rr;
    u32x4 xv[2][2];
#pragma unroll
    for (int bb = 0; bb < 2; ++bb)
#pragma unroll
      for (int h = 0; h < 2; ++h) xv[bb][h] = *(const GAS u32x4*)(xp + bb * 512 + h * 8);
    const u16* wp = p.wq[cbr] + (size_t)(256 * kh + 128 * hw + 16 * rr) * 4096 + kvh * 512 + 64 * cg + 8 * c;
    u32x4 w[16];
#pragma unroll
    for (int i = 0; i < WINQ; ++i) w[i] = ldg_w(wp + (size_t)i * 4096);
    __builtin_amdgcn_sched_barrier(0);
    MAKE_TAG();
    u16* outp = p.out; asm volatile("" : "+s"(outp));
    __builtin_amdgcn_sched_barrier(0);
  {
    float qa[2][8];
#pragma unroll
    for (int bb = 0; bb < 2; ++bb)
#pragma unroll
      for (int j = 0; j < 8; ++j) qa[bb][j] = 0.f;
#pragma unroll
    for (int t = 0; t < 8; ++t) {
      asm volatile("" :: "v"(w[2 * t]), "v"(w[2 * t + 1]));
      __builtin_amdgcn_sched_barrier(0);
#pragma unroll
      for (int i = 2 * t + WINQ; i < 2 * t + WINQ + 2; ++i) if (i < 16) w[i] = ldg_w(wp + (size_t)i * 4096);
      __builtin_amdgcn_sched_barrier(0);
#pragma unroll
      for (int j = 0; j < 8; ++j) {
        const u32 pr = __builtin_amdgcn_perm(w[2 * t + 1][j >> 1], w[2 * t][j >> 1], (j & 1) ? 0x07060302u : 0x05040100u);
#pragma unroll
        for (int bb = 0; bb < 2; ++bb) qa[bb][j] = dot2(pr, xv[bb][t >> 2][t & 3], qa[bb][j]);
      }
    }
    STAMP(1);
#pragma unroll
    for (int bb = 0; bb < 2; ++bb)
#pragma unroll
      for (int j = 0; j < 8; ++j) qa[bb][j] += dpp<ROR8>(qa[bb][j]);
    if (r < 8) {
#pragma unroll
      for (int bb = 0; bb < 2; ++bb)
#pragma unroll
        for (int h = 0; h < 2; ++h)
          *(LAS f32x4*)(L + L_QP + (((hw * 4 + g) * 8 + r) * 16 + bb * 8 + h * 4) * 4) = f32x4{qa[bb][4 * h], qa[bb][4 * h + 1], qa[bb][4 * h + 2], qa[bb][4 * h + 3]};
    }
  }
  LGKM0();
  const int skw = lane < 32 ? (lane >> 2) : 8 + ((lane >> 2) & 1);
  const int st_off = L_ST + (skw * 4 + (lane & 3)) * 16;
  const int hh = (jj >> 2) & 3;
  if (hw == 1) {
    if (lane == 0) *(volatile LAS u64*)(L + L_FLAG) = tag;
  } else {
    lds_wait_flag(L, L_FLAG, tag);
    const int piece = lane & 15, bb = (lane >> 4) & 1;
    f32x4 v = {0.f, 0.f, 0.f, 0.f};
#pragma unroll
    for (int i = 0; i < 8; ++i) v += *(LAS f32x4*)(L + L_QP + ((i * 8 + (piece >> 1)) * 16 + bb * 8 + (piece & 1) * 4) * 4);
    if (lane < 32) __builtin_amdgcn_raw_buffer_store_b128(btag4(v, tlo), rs, WS_MQ + (kvh * 2 + bb) * 8192 + kh * 4096 + cbr * 2048 + cg * 256 + piece * 16, 0, AUX_SC1);
    STAMP(2);
  }
  u32x4 wv[16];
  {
    const u16* wop = p.wo[cbr] + (size_t)(kvh * 512 + hh * 128 + hw * 64 + 2 * g) * 512 + (jj & 3) * 128 + 8 * r;
#pragma unroll
    for (int i = 0; i < 16; ++i) {
      if (i >= WINO) VMCNT(WINO - 1);
      wv[i] = ldg_w(wop + ((i >> 3) * 32 + 8 * (i & 3) + ((i >> 2) & 1)) * 512);
      __builtin_amdgcn_sched_barrier(0);
    }
  }
#define WO_TO_LDS() \
  _Pragma("unroll") for (int qd = 0; qd < 2; ++qd) \
    _Pragma("unroll") for (int j = 0; j < 8; ++j) *(LAS u32x4*)(L + L_WO + (((2 * hw + qd) * 8 + j) * 64 + lane) * 16) = tr_frag(wv + 8 * qd, j);
  if (hw == 1) {
    const int qoff = WS_MQ + (kvh * 2 + (jj >> 4)) * 8192 + lane * 16;
    u32x4 m[8];
#pragma unroll
    for (int i = 0; i < 8; ++i) m[i] = __builtin_amdgcn_raw_buffer_load_b128(rs, qoff + i * 1024, 0, AUX_SC1);
    __builtin_amdgcn_sched_barrier(0);
    WO_TO_LDS();
    __builtin_amdgcn_sched_barrier(0);
#pragma nounroll
    for (u32 spins = 0; spins < SPIN_CAP; ++spins) {
      bool miss[8]; bool any = false;
#pragma unroll
      for (int i = 0; i < 8; ++i) { miss[i] = __any(stale4(untag4(m[i], tlo))); any |= miss[i]; }
      if (!any) break;
      __builtin_amdgcn_s_sleep(POLL_SLEEP);
      CBAR();
#pragma unroll
      for (int i = 0; i < 8; ++i) if (miss[i]) m[i] = __builtin_amdgcn_raw_buffer_load_b128(rs, qoff + i * 1024, 0, AUX_SC1);
    }
    CBAR();
#pragma unroll
    for (int i = 0; i < 4; ++i) {
      const f32x4 v = __builtin_bit_cast(f32x4, untag4(m[i], tlo)) + __builtin_bit_cast(f32x4, untag4(m[i + 4], tlo));
      *(LAS u32x2*)(L + L_SQ + i * 512 + lane * 8) = u32x2{cvtpk(v[0], v[1]), cvtpk(v[2], v[3])};
    }
    LGKM0();
    if (lane == 0) *(volatile LAS u64*)(L + L_FLAG + 16) = tag;
    LGKM0();
    STAMP(3);
    return;
  }
  WO_TO_LDS();
  STAMP(18);
  {
    u32x4 sv;
#pragma nounroll
    for (u32 spins = 0; spins < SPIN_CAP; ++spins) {
      sv = *(volatile LAS u32x4*)(L + st_off);
      if (__all((sv[1] == tlo) & (sv[3] == thi))) break;
      __builtin_amdgcn_s_sleep(4);
    }
    CBAR();
    STAMP(4);
    const float mw = __uint_as_float(sv[0]);
    float M = mw;
    M = fmaxf(M, dpp<ROR4>(M)); M = fmaxf(M, dpp<ROR8>(M)); M = s16_max(M);
    const float beta = __builtin_amdgcn_exp2f(mw - M);
    float Ls = (lane >= 40) ? 0.f : __uint_as_float(sv[2]) * beta;
    Ls += dpp<ROR4>(Ls); Ls += dpp<ROR8>(Ls); Ls = s16_sum(Ls);
    const int e = (lane >> 5) * 4 + (lane & 3), sub = (lane >> 2) & 7;
    const u32x4 msg = {__float_as_uint(M), tlo, __float_as_uint(Ls), thi};
    if (lane < 40) *(LAS float*)(L + L_BETA + (lane & 3) * 48 + skw * 4) = beta;
    BAR();
    if (RDY_DELAY) __builtin_amdgcn_s_sleep(RDY_DELAY);
    if (sub == 0) __builtin_amdgcn_raw_buffer_store_b128(msg, rs, WS_MS + (kvh * 32 + jj) * 128 + e * 16, 0, AUX_SC1);
  }
  {
    u32x4 m = {0u, 0u, 0u, 0u};
#pragma nounroll
    for (u32 spins = 0; spins < SPIN_CAP; ++spins) {
      CBAR();
      if (lane < 32) m = __builtin_amdgcn_raw_buffer_load_b128(rs, WS_MS + (kvh * 32 + lane) * 128 + (cbr * 4 + hh) * 16, 0, AUX_SC1);
      const u32 ready = (u32)__ballot(((m[1] ^ tlo) | (m[3] ^ thi)) == 0);
      if (lane == 0) *(volatile LAS u32x2*)(L + L_FLAG + 32) = u32x2{ready, thi};
      if (ready == 0xFFFFFFFFu) break;
    }
    const float ms = __uint_as_float(m[0]);
    float Mg = ms;
    Mg = fmaxf(Mg, dpp<XOR1>(Mg)); Mg = fmaxf(Mg, dpp<XOR2>(Mg)); Mg = fmaxf(Mg, dpp<ROR4>(Mg)); Mg = fmaxf(Mg, dpp<ROR8>(Mg));
    const float al = __builtin_amdgcn_exp2f(ms - Mg);
    float Lg = __uint_as_float(m[2]) * al;
    Lg += dpp<XOR1>(Lg); Lg += dpp<XOR2>(Lg); Lg += dpp<ROR4>(Lg); Lg += dpp<ROR8>(Lg);
    const float wgt = al * __builtin_amdgcn_rcpf(Lg);
    if (lane < 32) *(volatile LAS u32*)(L + L_WT + (lane & 7) * 16 + (lane >> 3) * 4) = btag(wgt, (tlo >> ((lane >> 3) * 8)) & 255u);
  }
  STAMP(16);
  BAR();
  {
    const int fbr = (blk >> 2) & 1, fcq = blk & 3, fb = blk >> 7, fe = (blk >> 3) & 15;
    const int pi = lane >> 1;
    const int off = WS_H2 + ((pi & 7) + 8 * (16 * fbr + 4 * (pi >> 3) + fcq)) * H2_STR + fb * 512 + fe * 32 + (lane & 1) * 16;
    u32x4 a;
#pragma nounroll
    for (u32 spins = 0; spins < SPIN_CAP; ++spins) {
      CBAR();
      a = untag4(__builtin_amdgcn_raw_buffer_load_b128(rs, off, 0, AUX_SC1), tlo);
      if (!__any(stale4(a))) break;
    }
    float x[2];
#pragma unroll
    for (int e2 = 0; e2 < 2; ++e2) {
      auto t = __builtin_amdgcn_permlane32_swap(a[e2], a[e2 + 2], false, false);
      float v = __uint_as_float(t[0]) + __uint_as_float(t[1]);
      v = s16_sum(v); v += dpp<ROR8>(v); v += dpp<ROR4>(v); v += dpp<XOR2>(v);
      x[e2] = v;
    }
    if ((lane & 30) == 0) *(GAS u32*)(outp + fb * 1024 + fbr * 512 + fcq * 128 + 8 * (4 * (fe & 3) + 2 * (lane & 1) + (lane >> 5)) + 2 * (fe >> 2)) = cvtpk(x[0], x[1]);
    if (blk == 0 && lane == 0) { const u32x2 tv = {tlo, thi}; __builtin_amdgcn_raw_buffer_store_b64(tv, rs, WS_E, 0, AUX_SC1); }
  }
  STAMP(13);
}

extern "C" void kernel_launch(void* const* d_in, const int* in_sizes, int n_in, void* d_out, int out_size, void* d_ws, size_t ws_size, hipStream_t stream) {
  Params p;
  for (int br = 0; br < 2; ++br) {
    p.x[br] = (const u16*)d_in[br * 5 + 0]; p.k[br] = (const u16*)d_in[br * 5 + 1]; p.v[br] = (const u16*)d_in[br * 5 + 2];
    p.wq[br] = (const u16*)d_in[br * 5 + 3]; p.wo[br] = (const u16*)d_in[br * 5 + 4];
  }
  p.out = (u16*)d_out; p.ws = (unsigned char*)d_ws;
  fused_decode<<<256, 768, 0, stream>>>(p);
}
```

```cpp
#include <hip/hip_runtime.h>
#include <stdint.h>

typedef unsigned short u16;
typedef unsigned int u32;
typedef unsigned long long u64;
typedef u32 u32x2 __attribute__((ext_vector_type(2)));
typedef u32 u32x4 __attribute__((ext_vector_type(4)));
typedef float f32x2 __attribute__((ext_vector_type(2)));
typedef float f32x4 __attribute__((ext_vector_type(4)));
typedef short s16x4 __attribute__((ext_vector_type(4)));
typedef short s16x8 __attribute__((ext_vector_type(8)));
typedef __bf16 bf16x2 __attribute__((ext_vector_type(2)));
#define GAS __attribute__((address_space(1)))
#define LAS __attribute__((address_space(3)))

#define WIN 2
#define WMAX 4
#define PACE_0 55
#define PACE_K 32
#define PACE_V 34
#define GRADE 40
#define Q_TRIG 12
#define WO_EARLY 16
#define WO_LEFT 0
#define H2_DELAY 20

struct Params {
  const u16* x[2]; const u16* k[2]; const u16* v[2]; const u16* wq[2]; const u16* wo[2];
  u16* out; unsigned char* ws;
};

constexpr int WS_E = 0, WS_MS = 1 << 15, WS_MQ = 1 << 16, WS_H2 = 3 << 16, WS_H1 = 1 << 19, WS_BYTES = 2 << 20;
constexpr int H1_STR = 4096 + 128, H2_STR = 1024 + 128;
constexpr int AUX_SC1 = 16, AUX_NT = 2;
constexpr u32 SPIN_CAP = 1u << 20;

#define STAMP(i)

__device__ __forceinline__ u32x4 ldg_nt(const u16* ptr) { return __builtin_nontemporal_load((const GAS u32x4*)ptr); }
__device__ __forceinline__ f32x4 mfma16(u32x4 a, u32x4 b, f32x4 c) {
  return __builtin_amdgcn_mfma_f32_16x16x32_bf16(__builtin_bit_cast(s16x8, a), __builtin_bit_cast(s16x8, b), c, 0, 0, 0);
}
__device__ __forceinline__ u32 cvtpk(float lo, float hi) {
  f32x2 v = {lo, hi};
  return __builtin_bit_cast(u32, __builtin_convertvector(v, bf16x2));
}
__device__ __forceinline__ float lo_f(u32 v) { return __builtin_bit_cast(float, v << 16); }
__device__ __forceinline__ float hi_f(u32 v) { return __builtin_bit_cast(float, v & 0xffff0000u); }
__device__ __forceinline__ float dot2(u32 a, u32 b, float acc) {
  return __builtin_amdgcn_fdot2_f32_bf16(__builtin_bit_cast(bf16x2, a), __builtin_bit_cast(bf16x2, b), acc, false);
}
template <int CTRL> __device__ __forceinline__ float dpp(float x) {
  return __builtin_bit_cast(float, __builtin_amdgcn_mov_dpp(__builtin_bit_cast(int, x), CTRL, 0xf, 0xf, true));
}
constexpr int XOR1 = 0xB1, XOR2 = 0x4E, ROR4 = 0x124, ROR8 = 0x128;
__device__ __forceinline__ float s16_max(float x) {
  auto s = __builtin_amdgcn_permlane16_swap(__float_as_uint(x), __float_as_uint(x), false, false);
  return fmaxf(__uint_as_float(s[0]), __uint_as_float(s[1]));
}
__device__ __forceinline__ float s32_max(float x) {
  auto t = __builtin_amdgcn_permlane32_swap(__float_as_uint(x), __float_as_uint(x), false, false);
  return fmaxf(__uint_as_float(t[0]), __uint_as_float(t[1]));
}
__device__ __forceinline__ float s16_sum(float x) {
  auto s = __builtin_amdgcn_permlane16_swap(__float_as_uint(x), __float_as_uint(x), false, false);
  return __uint_as_float(s[0]) + __uint_as_float(s[1]);
}
__device__ __forceinline__ float s32_sum(float x) {
  auto t = __builtin_amdgcn_permlane32_swap(__float_as_uint(x), __float_as_uint(x), false, false);
  return __uint_as_float(t[0]) + __uint_as_float(t[1]);
}
__device__ __forceinline__ u32 btag(float v, u32 tb) { return ((__float_as_uint(v) + 0x80u) & 0xFFFFFF00u) | tb; }
__device__ __forceinline__ u32x4 btag4(f32x4 v, u32 t) { u32x4 r = {btag(v[0], t & 255u), btag(v[1], (t >> 8) & 255u), btag(v[2], (t >> 16) & 255u), btag(v[3], t >> 24)}; return r; }
__device__ __forceinline__ u32x4 untag4(u32x4 v, u32 t) { u32x4 r = {v[0] ^ (t & 255u), v[1] ^ ((t >> 8) & 255u), v[2] ^ ((t >> 16) & 255u), v[3] ^ (t >> 24)}; return r; }
__device__ __forceinline__ u32 or4(u32x4 v) { return v[0] | v[1] | v[2] | v[3]; }
__device__ __forceinline__ u32x4 ntag4(f32x4 v, u32 t) {
  u32x4 r;
#pragma unroll
  for (int i = 0; i < 4; ++i) r[i] = ((__float_as_uint(v[i]) + 8u) & ~15u) | ((t >> (8 * i)) & 15u);
  return r;
}
__device__ __forceinline__ u64 mix64(u64 z) {
  z += 0x9E3779B97F4A7C15ull; z = (z ^ (z >> 30)) * 0xBF58476D1CE4E5B9ull; z = (z ^ (z >> 27)) * 0x94D049BB133111EBull; return z ^ (z >> 31);
}
#define BAR() do { asm volatile("s_waitcnt lgkmcnt(0)" ::: "memory"); __builtin_amdgcn_s_barrier(); asm volatile("" ::: "memory"); } while (0)
#define VMCNT(n) asm volatile("s_waitcnt vmcnt(%0)" :: "i"(n) : "memory")
#define LGKM0() asm volatile("s_waitcnt lgkmcnt(0)" ::: "memory")
#define CBAR() asm volatile("" ::: "memory")
#define SB() __builtin_amdgcn_sched_barrier(0)

#define RP_MASK(U, M) \
  "v_or3_b32 v144, %[" U "0], %[" U "1], %[" U "2]\n\tv_bitop3_b32 v144, v144, s73, %[" U "3] bitop3:0xc8\n\tv_cmp_ne_u32 vcc, 0, v144\n\ts_wqm_b64 " M ", vcc\n\t"
#define RP_LOAD(M, R, O, L) \
  "s_mov_b64 exec, " M "\n\ts_cbranch_execz " L "f\n\tbuffer_load_dwordx4 " R ", %[" O "], %[rs], 0 offen sc1\n" L ":\n\t"
#define RP_TAKE(U, M, R0, R1, R2, R3) \
  "s_mov_b64 exec, " M "\n\tv_xor_b32 %[" U "0], %[b0], " R0 "\n\tv_xor_b32 %[" U "1], %[b1], " R1 "\n\tv_xor_b32 %[" U "2], %[b2], " R2 "\n\tv_xor_b32 %[" U "3], %[b3], " R3 "\n\t"
__device__ __forceinline__ void repoll4(u32x4& a, u32x4& b, u32x4& c, u32x4& d, int oa, int ob, int oc, int od, u32x4 rsrc, u32 tlo) {
  u32 a0 = a[0], a1 = a[1], a2 = a[2], a3 = a[3], b0 = b[0], b1 = b[1], b2 = b[2], b3 = b[3];
  u32 c0 = c[0], c1 = c[1], c2 = c[2], c3 = c[3], d0 = d[0], d1 = d[1], d2 = d[2], d3 = d[3];
  asm volatile(
    "s_mov_b32 s72, 0x100000\n\t"
    "s_movk_i32 s73, 0xff\n"
    "1:\n\t"
    RP_MASK("p", "s[64:65]") RP_MASK("q", "s[66:67]") RP_MASK("r", "s[68:69]") RP_MASK("t", "s[70:71]")
    "s_or_b64 vcc, s[64:65], s[66:67]\n\t"
    "s_or_b64 vcc, vcc, s[68:69]\n\t"
    "s_or_b64 vcc, vcc, s[70:71]\n\t"
    "s_cbranch_scc0 2f\n\t"
    RP_LOAD("s[64:65]", "v[128:131]", "oa", "10") RP_LOAD("s[66:67]", "v[132:135]", "ob", "11")
    RP_LOAD("s[68:69]", "v[136:139]", "oc", "12") RP_LOAD("s[70:71]", "v[140:143]", "od", "13")
    "s_waitcnt vmcnt(0)\n\t"
    RP_TAKE("p", "s[64:65]", "v128", "v129", "v130", "v131") RP_TAKE("q", "s[66:67]", "v132", "v133", "v134", "v135")
    RP_TAKE("r", "s[68:69]", "v136", "v137", "v138", "v139") RP_TAKE("t", "s[70:71]", "v140", "v141", "v142", "v143")
    "s_mov_b64 exec, -1\n\t"
    "s_sub_u32 s72, s72, 1\n\t"
    "s_cbranch_scc0 1b\n"
    "2:"
    : [p0] "+v"(a0), [p1] "+v"(a1), [p2] "+v"(a2), [p3] "+v"(a3), [q0] "+v"(b0), [q1] "+v"(b1), [q2] "+v"(b2), [q3] "+v"(b3),
      [r0] "+v"(c0), [r1] "+v"(c1), [r2] "+v"(c2), [r3] "+v"(c3), [t0] "+v"(d0), [t1] "+v"(d1), [t2] "+v"(d2), [t3] "+v"(d3)
    : [oa] "v"(oa), [ob] "v"(ob), [oc] "v"(oc), [od] "v"(od), [rs] "s"(rsrc),
      [b0] "s"(tlo & 255u), [b1] "s"((tlo >> 8) & 255u), [b2] "s"((tlo >> 16) & 255u), [b3] "s"(tlo >> 24)
    : "memory", "vcc", "scc", "v128", "v129", "v130", "v131", "v132", "v133", "v134", "v135", "v136", "v137", "v138", "v139", "v140", "v141", "v142", "v143", "v144",
      "s64", "s65", "s66", "s67", "s68", "s69", "s70", "s71", "s72", "s73");
  a = u32x4{a0, a1, a2, a3}; b = u32x4{b0, b1, b2, b3}; c = u32x4{c0, c1, c2, c3}; d = u32x4{d0, d1, d2, d3};
}
__device__ __forceinline__ void repoll1(u32x4& a, int oa, u32x4 rsrc, u32 tlo) {
  u32 a0 = a[0], a1 = a[1], a2 = a[2], a3 = a[3];
  asm volatile(
    "s_mov_b32 s72, 0x100000\n\t"
    "s_movk_i32 s73, 0xff\n"
    "1:\n\t"
    RP_MASK("p", "exec")
    "s_cbranch_scc0 2f\n\t"
    "buffer_load_dwordx4 v[128:131], %[oa], %[rs], 0 offen sc1\n\t"
    "s_sub_u32 s72, s72, 1\n\t"
    "s_waitcnt vmcnt(0)\n\t"
    "v_xor_b32 %[p0], %[b0], v128\n\tv_xor_b32 %[p1], %[b1], v129\n\tv_xor_b32 %[p2], %[b2], v130\n\tv_xor_b32 %[p3], %[b3], v131\n\t"
    "s_cbranch_scc0 1b\n"
    "2:\n\t"
    "s_mov_b64 exec, -1"
    : [p0] "+v"(a0), [p1] "+v"(a1), [p2] "+v"(a2), [p3] "+v"(a3)
    : [oa] "v"(oa), [rs] "s"(rsrc), [b0] "s"(tlo & 255u), [b1] "s"((tlo >> 8) & 255u), [b2] "s"((tlo >> 16) & 255u), [b3] "s"(tlo >> 24)
    : "memory", "vcc", "scc", "v128", "v129", "v130", "v131", "v144", "s72", "s73");
  a = u32x4{a0, a1, a2, a3};
}

constexpr int L_WO = 0;
constexpr int L_RED = 32768;
constexpr int L_SQ = L_RED + 20480;
constexpr int L_ST = L_SQ + 2048;
constexpr int L_BETA = L_ST + 640;
constexpr int L_QP = L_BETA + 320;
constexpr int L_O2 = L_QP + 4096;
constexpr int L_WT = L_O2 + 512;
constexpr int L_FLAG = L_WT + 128;
constexpr int F_QP = 0, F_Q = 8, F_QTRIG = 16, F_GO = 24, F_WO = 32, F_VD = 48;
constexpr int L_BYTES = L_FLAG + F_VD + 80;

template <int SLEEP> __device__ __forceinline__ void lds_wait_flag(LAS unsigned char* L, int off, u64 tag) {
#pragma nounroll
  for (u32 spins = 0; spins < SPIN_CAP; ++spins) {
    if (*(volatile LAS u64*)(L + L_FLAG + off) == tag) break;
    if (SLEEP) __builtin_amdgcn_s_sleep(SLEEP);
  }
  CBAR();
}
#define SET_FLAG(off) do { if (lane == 0) *(volatile LAS u64*)(L + L_FLAG + (off)) = tag; } while (0)

#define MAKE_TAG() \
  SB(); \
  u64 E; asm volatile("s_load_dwordx2 %0, %1, 0x0\n\ts_waitcnt lgkmcnt(0)" : "=s"(E) : "s"(p.ws) : "memory"); \
  const u64 mixed = mix64(E); \
  const u32 tx_ = ((u32)mixed ^ (u32)E) & 0x0F0F0F0Fu; \
  const u32 tlo = (u32)mixed ^ (((tx_ - 0x01010101u) & ~tx_ & 0x80808080u) >> 4);     \
  const u32 ty_ = (u32)(mixed >> 32) ^ (u32)(E >> 32); \
  const u32 thi = (u32)(mixed >> 32) ^ ((ty_ - 0x01010101u) & ~ty_ & 0x80808080u); \
  const u64 tag = ((u64)thi << 32) | tlo; \
  SB();

__global__ void __launch_bounds__(768, 3) fused_decode(Params p) {
  __shared__ __attribute__((aligned(16))) unsigned char smem[L_BYTES];
  LAS unsigned char* L = (LAS unsigned char*)smem;
  const int blk = blockIdx.x, tid = threadIdx.x, lane = tid & 63;
  const int wave = __builtin_amdgcn_readfirstlane(tid >> 6);
  const int kvh = blk & 7, jj = blk >> 3;
  const int g = lane >> 4, r = lane & 15;
  asm volatile("" : "+s"(p.x[0]), "+s"(p.x[1]), "+s"(p.k[0]), "+s"(p.k[1]), "+s"(p.v[0]), "+s"(p.v[1]), "+s"(p.wq[0]), "+s"(p.wq[1]), "+s"(p.wo[0]), "+s"(p.wo[1]), "+s"(p.out), "+s"(p.ws));
  const __amdgpu_buffer_rsrc_t rs = __builtin_amdgcn_make_buffer_rsrc(p.ws, 0, WS_BYTES, 0x00020000);
  const u32x4 rsv = {(u32)(u64)p.ws, (u32)((u64)p.ws >> 32) & 0xffffu, (u32)WS_BYTES, 0x00020000u};
  constexpr float LOG2E = 1.4426950408889634f;
  const int cbr = jj >> 4, hh = (jj >> 2) & 3;

  if (wave >= 2) {
    STAMP(14);
    const int kw = wave - 2, br = kw >= 8 ? 1 : 0;
    const int key0 = br ? 6144 + 128 * (jj & 15) + 64 * (kw - 8) : 512 * (jj & 15) + 64 * kw;
    const size_t base = ((size_t)((jj >> 4) * 8 + kvh) * 8192 + key0) * 128;
    const int rot = (kw + jj) & 3;
    const u16* Kp = (br ? p.k[1] : p.k[0]) + base + (r >> 2) * 128 + (4 * g + (r & 3)) * 8;
    const u16* Vp = (br ? p.v[1] : p.v[0]) + base + lane * 8;
    u32x4 Kf[16], Vf[16];
#define KLOAD(n) do { SB(); Kf[n] = ldg_nt(Kp + (((n) >> 2) ^ rot) * 2048 + ((n) & 3) * 512); SB(); } while (0)
#define VLOAD(n) do { SB(); Vf[n] = ldg_nt(Vp + (((n) >> 2) ^ rot) * 2048 + ((n) & 3) * 512); SB(); } while (0)
#define THROTTLE(due) do { VMCNT(WMAX - 1); const u32 t_ = (u32)__builtin_amdgcn_s_memrealtime() - pt0; \
    if (t_ < (u32)((due) + GRADE)) { VMCNT(WIN); if (t_ < (u32)(due)) VMCNT(WIN - 1); } } while (0)
#define VSTEP(n) do { THROTTLE(PACE_0 + 16 * PACE_K + (n) * PACE_V); VLOAD(n); } while (0)
#pragma unroll
    for (int n = 0; n < WIN; ++n) KLOAD(n);
    MAKE_TAG();
    const u32 pt0 = (u32)__builtin_amdgcn_s_memrealtime();
#pragma unroll
    for (int n = WIN; n < 16; ++n) {
      THROTTLE(PACE_0 + n * PACE_K);
      KLOAD(n);
      if (n == Q_TRIG && kw == 0) SET_FLAG(F_QTRIG);
    }
    STAMP(17);
    VSTEP(0); VSTEP(1);
    lds_wait_flag<1>(L, F_Q, tag);
    STAMP(6);
    const u32x4 Qf = *(LAS u32x4*)(L + L_SQ + br * 1024 + (r >> 2) * 256 + (4 * g + (r & 3)) * 16);
    float sc[16];
#pragma unroll
    for (int c = 0; c < 4; ++c) {
      f32x4 d[4];
#pragma unroll
      for (int i = 0; i < 4; ++i) d[i] = mfma16(Kf[4 * c + i], Qf, f32x4{0.f, 0.f, 0.f, 0.f});
#pragma unroll
      for (int i = 0; i < 4; ++i) sc[4 * c + i] = (dpp<0x00>(d[i][0]) + dpp<0x55>(d[i][1])) + (dpp<0xAA>(d[i][2]) + dpp<0xFF>(d[i][3]));
#pragma unroll
      for (int i = 4 * c; i < 4 * c + 4; i += 2) {
        const u32 a0 = cvtpk(sc[i], sc[i + 1]);
        const u32 c0 = cvtpk(lo_f(a0) * 0.08838834764831845f, hi_f(a0) * 0.08838834764831845f);
        sc[i] = lo_f(c0); sc[i + 1] = hi_f(c0);
      }
      VSTEP(2 + c);
    }
    float mx = sc[0];
#pragma unroll
    for (int i = 1; i < 16; ++i) mx = fmaxf(mx, sc[i]);
    mx = s32_max(s16_max(mx));
    const float M2 = mx * LOG2E;
    VSTEP(6);
    float ls = 0.f;
    u32x4 Pf[2];
#pragma unroll
    for (int u = 0; u < 2; ++u) {
#pragma unroll
      for (int i = 8 * u; i < 8 * u + 8; ++i) { sc[i] = __builtin_amdgcn_exp2f(__builtin_fmaf(sc[i], LOG2E, -M2)); ls += sc[i]; }
#pragma unroll
      for (int t = 0; t < 4; ++t) Pf[u][t] = cvtpk(sc[8 * u + 2 * t], sc[8 * u + 2 * t + 1]);
      if (u == 1) {
        ls = s32_sum(s16_sum(ls));
        if ((lane & 51) == 0) *(LAS u32x4*)(L + L_ST + (kw * 4 + (lane >> 2)) * 16) = u32x4{__float_as_uint(M2), tlo, __float_as_uint(ls), thi};
      }
      VSTEP(7 + u);
    }
    STAMP(7);
    VSTEP(9); VSTEP(10); VSTEP(11);
    f32x4 acc[8];
    u32x4 F[8];
#pragma unroll
    for (int j = 0; j < 8; ++j) {
#pragma unroll
      for (int t = 0; t < 4; ++t) F[j][t] = __builtin_amdgcn_perm(Vf[2 * t + 1][j >> 1], Vf[2 * t][j >> 1], (j & 1) ? 0x07060302u : 0x05040100u);
      acc[j] = mfma16(Pf[0], F[j], f32x4{0.f, 0.f, 0.f, 0.f});
      if (j == 3) VSTEP(12);
    }
    VSTEP(13); VSTEP(14); VSTEP(15);
    SET_FLAG(F_VD + kw * 8);
    STAMP(9);
    float beta;
#pragma nounroll
    for (u32 spins = 0; spins < SPIN_CAP; ++spins) {
      const u32x2 bv = *(volatile LAS u32x2*)(L + L_BETA + (kw * 4 + g) * 8);
      beta = __uint_as_float(bv[0]);
      if (__all(bv[1] == thi)) break;
      __builtin_amdgcn_s_sleep(1);
    }
    if (kw >= 4 && kw < 8) __builtin_amdgcn_s_setprio(3);
    u32 sumr = (u32)(size_t)(L + L_RED + ((kw >> 1) & 1) * 16384 + (kw & 1) * 1024 + lane * 16);
    int h1off = WS_H1 + blk * H1_STR + ((kw >> 1) & 1) * 2048 + g * 512 + (kw & 1) * 256 + r * 16;
    asm volatile("" : "+v"(sumr), "+v"(h1off));
#pragma unroll
    for (int t = 0; t < 4; ++t) {
#pragma unroll
      for (int j = 0; j < 8; ++j) F[j][t] = __builtin_amdgcn_perm(Vf[8 + 2 * t + 1][j >> 1], Vf[8 + 2 * t][j >> 1], (j & 1) ? 0x07060302u : 0x05040100u);
      if (t < 3) SB();
    }
#pragma unroll
    for (int j = 0; j < 8; ++j) acc[j] = mfma16(Pf[1], F[j], acc[j]);
    *(LAS f32x4*)(L + L_RED + kw * 2048 + lane * 16) = f32x4{acc[0][0], acc[1][0], acc[2][0], acc[3][0]} * beta;
    *(LAS f32x4*)(L + L_RED + kw * 2048 + 1024 + lane * 16) = f32x4{acc[4][0], acc[5][0], acc[6][0], acc[7][0]} * beta;
    STAMP(8);
    BAR();
    STAMP(10);
    if (__builtin_expect(kw >= 4, 1)) {
      if (__builtin_expect(kw < 6, 1)) {
        f32x4 t[8];
#pragma unroll
        for (int w = 0; w < 8; ++w) t[w] = *(LAS f32x4*)(size_t)(sumr + w * 2048);
        SB();
        const f32x4 v = ((t[0] + t[1]) + (t[2] + t[3])) + ((t[4] + t[5]) + (t[6] + t[7]));
        __builtin_amdgcn_raw_buffer_store_b128(btag4(v, tlo), rs, h1off, 0, AUX_SC1);
      } else if (kw < 8) {
        const f32x4 v = *(LAS f32x4*)(size_t)sumr + *(LAS f32x4*)(size_t)(sumr + 2048);
        __builtin_amdgcn_raw_buffer_store_b128(btag4(v, tlo), rs, h1off, 0, AUX_SC1);
      }
      return;
    }
    u32 o2w = (u32)(size_t)(L + L_O2 + (lane >> 5) * 256 + kw * 64 + (lane & 7) * 8 + ((lane >> 4) & 1) * 4);
    u32 o2r = (u32)(size_t)(L + L_O2 + ((r >> 2) & 1) * 256 + g * 16);
    int h2off = lane < 32 ? WS_H2 + blk * H2_STR + g * 512 + kw * 128 + r * 8 : WS_BYTES;
    u32 tb0 = (tlo >> ((r & 1) * 16)) & 255u, tb1 = (tlo >> ((r & 1) * 16 + 8)) & 255u;
    int o0 = WS_H1 + (kvh + 8 * (lane >> 3)) * H1_STR + cbr * 2048 + hh * 512 + kw * 128 + (lane & 7) * 16;
    int o1 = o0 + 64 * H1_STR, o2 = o0 + 128 * H1_STR, o3 = o0 + 192 * H1_STR;
    u32 wr0 = (u32)(size_t)(L + L_WO + (8 * g + (r >> 2)) * 256 + (((4 * kw + ((r & 3) >> 1)) ^ (2 * ((r >> 2) | ((g & 1) << 2)))) * 16) + (r & 1) * 8);
    u32 wr1 = wr0 ^ 32u;
    asm volatile("" : "+v"(o2w), "+v"(o2r), "+v"(h2off), "+v"(tb0), "+v"(tb1), "+v"(o0), "+v"(o1), "+v"(o2), "+v"(o3), "+v"(wr0), "+v"(wr1));
    u32x4 wu;
#pragma nounroll
    for (u32 spins = 0; spins < SPIN_CAP; ++spins) {
      wu = untag4(*(volatile LAS u32x4*)(L + L_WT + (lane >> 3) * 16), tlo);
      if (!__any((or4(wu) & 255u) != 0)) break;
      __builtin_amdgcn_s_sleep(1);
    }
    lds_wait_flag<0>(L, WO_LEFT ? F_GO : F_WO, tag);
    u32x4 u0 = __builtin_amdgcn_raw_buffer_load_b128(rs, o0, 0, AUX_SC1), u1 = __builtin_amdgcn_raw_buffer_load_b128(rs, o1, 0, AUX_SC1);
    u32x4 u2 = __builtin_amdgcn_raw_buffer_load_b128(rs, o2, 0, AUX_SC1), u3 = __builtin_amdgcn_raw_buffer_load_b128(rs, o3, 0, AUX_SC1);
    SB();
    if (WO_LEFT) lds_wait_flag<0>(L, F_WO, tag);
    u32x4 wf[8];
#pragma unroll
    for (int i = 0; i < 8; ++i) {
      const u32 a = ((i & 1) ? wr1 : wr0) + (i >> 1) * 8192;
      const u32x2 h0 = __builtin_bit_cast(u32x2, __builtin_amdgcn_ds_read_tr16_b64_v4i16((LAS s16x4*)(size_t)a));
      const u32x2 h1 = __builtin_bit_cast(u32x2, __builtin_amdgcn_ds_read_tr16_b64_v4i16((LAS s16x4*)(size_t)(a + 1024)));
      wf[i] = u32x4{h0[0], h0[1], h1[0], h1[1]};
    }
    SB();
    u0 = untag4(u0, tlo); u1 = untag4(u1, tlo); u2 = untag4(u2, tlo); u3 = untag4(u3, tlo);
    if (__builtin_expect(__any(((or4(u0) | or4(u1) | or4(u2) | or4(u3)) & 255u) != 0), 0)) repoll4(u0, u1, u2, u3, o0, o1, o2, o3, rsv, tlo);
    STAMP(11);
    {
      float x[4];
#pragma unroll
      for (int e = 0; e < 4; ++e) {
        float s0 = __uint_as_float(u0[e]) * __uint_as_float(wu[0]), s1 = __uint_as_float(u2[e]) * __uint_as_float(wu[2]);
        asm("" : "+v"(s0)); asm("" : "+v"(s1));
        s0 = __builtin_fmaf(__uint_as_float(u1[e]), __uint_as_float(wu[1]), s0); s1 = __builtin_fmaf(__uint_as_float(u3[e]), __uint_as_float(wu[3]), s1);
        asm("" : "+v"(s0)); asm("" : "+v"(s1));
        auto t = __builtin_amdgcn_permlane32_swap(__float_as_uint(s0), __float_as_uint(s1), false, false);
        x[e] = __uint_as_float(t[0]) + __uint_as_float(t[1]);
      }
      float y[2];
#pragma unroll
      for (int e = 0; e < 2; ++e) {
        auto t = __builtin_amdgcn_permlane16_swap(__float_as_uint(x[e]), __float_as_uint(x[e + 2]), false, false);
        y[e] = __uint_as_float(t[0]) + __uint_as_float(t[1]);
        asm("" : "+v"(y[e]));
        y[e] += dpp<ROR8>(y[e]);
      }
      *(LAS u32*)(size_t)o2w = cvtpk(y[0], y[1]);
    }
    BAR();
    STAMP(19);
    {
      u32x4 Of[4];
#pragma unroll
      for (int qd = 0; qd < 4; ++qd) Of[qd] = *(LAS u32x4*)(size_t)(o2r + qd * 64);
      SB();
      f32x4 d0 = {0.f, 0.f, 0.f, 0.f}, d1 = {0.f, 0.f, 0.f, 0.f};
#pragma unroll
      for (int qd = 0; qd < 4; ++qd) {
        d0 = mfma16(Of[qd], wf[2 * qd], d0);
        d1 = mfma16(Of[qd], wf[2 * qd + 1], d1);
      }
      __builtin_amdgcn_raw_buffer_store_b64(u32x2{btag(d0[0], tb0), btag(d1[0], tb1)}, rs, h2off, 0, AUX_SC1);
    }
    return;
  }

  STAMP(0);
  __builtin_amdgcn_s_setprio(3);
  const int hw = wave;
  const int kh = jj & 1, cg = (jj >> 1) & 7;
  u32x4 xv[2][2], w[16];
  {
    const int rr = lane >> 3, c = lane & 7;
    const u16* xp = (cbr ? p.x[1] : p.x[0]) + 256 * kh + 128 * hw + 16 * rr;
    const u16* wp = (cbr ? p.wq[1] : p.wq[0]) + (size_t)(256 * kh + 128 * hw + 16 * rr) * 4096 + kvh * 512 + 64 * cg + 8 * c;
#pragma unroll
    for (int i = 0; i < 4; ++i) xv[i >> 1][i & 1] = *(const GAS u32x4*)(xp + (i >> 1) * 512 + (i & 1) * 8);
    SB();
#pragma unroll
    for (int i = 0; i < 16; ++i) w[i] = ldg_nt(wp + (size_t)i * 4096);
  }
  MAKE_TAG();
  {
    float qa[2][8];
#pragma unroll
    for (int bb = 0; bb < 2; ++bb)
#pragma unroll
      for (int j = 0; j < 8; ++j) qa[bb][j] = 0.f;
#pragma unroll
    for (int t = 0; t < 8; ++t)
#pragma unroll
      for (int j = 0; j < 8; ++j) {
        const u32 pr = __builtin_amdgcn_perm(w[2 * t + 1][j >> 1], w[2 * t][j >> 1], (j & 1) ? 0x07060302u : 0x05040100u);
#pragma unroll
        for (int bb = 0; bb < 2; ++bb) qa[bb][j] = dot2(pr, xv[bb][t >> 2][t & 3], qa[bb][j]);
      }
    STAMP(1);
#pragma unroll
    for (int bb = 0; bb < 2; ++bb)
#pragma unroll
      for (int j = 0; j < 8; ++j) qa[bb][j] += dpp<ROR8>(qa[bb][j]);
    if (r < 8) {
#pragma unroll
      for (int bb = 0; bb < 2; ++bb)
#pragma unroll
        for (int h = 0; h < 2; ++h)
          *(LAS f32x4*)(L + L_QP + (((hw * 4 + g) * 8 + r) * 16 + bb * 8 + h * 4) * 4) = f32x4{qa[bb][4 * h], qa[bb][4 * h + 1], qa[bb][4 * h + 2], qa[bb][4 * h + 3]};
    }
  }
  LGKM0();
  if (hw == 1) {
    SET_FLAG(F_QP);
    lds_wait_flag<4>(L, F_QTRIG, tag);
    const int qoff = WS_MQ + (kvh * 2 + (jj >> 4)) * 8192 + lane * 16;
    u32x4 m[8];
#pragma nounroll
    for (u32 spins = 0; spins < SPIN_CAP; ++spins) {
      CBAR();
      u32 bad = 0;
#pragma unroll
      for (int i = 0; i < 8; ++i) m[i] = __builtin_amdgcn_raw_buffer_load_b128(rs, qoff + i * 1024, 0, AUX_SC1);
#pragma unroll
      for (int i = 0; i < 8; ++i) { m[i] = untag4(m[i], tlo & 0x0F0F0F0Fu); bad |= or4(m[i]); }
      if (!__any((bad & 15u) != 0)) break;
      __builtin_amdgcn_s_sleep(16);
    }
    CBAR();
#pragma unroll
    for (int i = 0; i < 4; ++i) {
      const f32x4 v = __builtin_bit_cast(f32x4, m[i]) + __builtin_bit_cast(f32x4, m[i + 4]);
      *(LAS u32x2*)(L + L_SQ + i * 512 + lane * 8) = u32x2{cvtpk(v[0], v[1]), cvtpk(v[2], v[3])};
    }
    LGKM0();
    SET_FLAG(F_Q);
    STAMP(3);
#pragma nounroll
    for (u32 spins = 0; spins < SPIN_CAP; ++spins) {
      if (__all(*(volatile LAS u64*)(L + L_FLAG + F_VD + (r < 10 ? r : 0) * 8) == tag)) break;
      __builtin_amdgcn_s_sleep(2);
    }
    CBAR();
    STAMP(5);
    const u16* wop = (cbr ? p.wo[1] : p.wo[0]) + (size_t)(kvh * 512 + hh * 128 + g) * 512 + (jj & 3) * 128;
#pragma unroll
    for (int i = 0; i < 32; ++i) {
      const int dim = 64 * ((i >> 3) & 1) + 8 * (i & 7) + 4 * (i >> 4);
      const int chunk = r ^ (2 * (g | (((i >> 1) & 1) << 2)));
      if (i == WO_EARLY) __builtin_amdgcn_s_barrier();
      __builtin_amdgcn_global_load_lds((const GAS void*)(wop + dim * 512 + chunk * 8), (LAS void*)(L + L_WO + i * 1024), 16, 0, AUX_NT);
    }
    if (WO_LEFT) { VMCNT(WO_LEFT); SET_FLAG(F_GO); }
    VMCNT(0);
    SET_FLAG(F_WO);
    LGKM0();
    STAMP(18);
    return;
  }
  lds_wait_flag<1>(L, F_QP, tag);
  {
    const int piece = lane & 15, bb = (lane >> 4) & 1;
    f32x4 v = {0.f, 0.f, 0.f, 0.f};
#pragma unroll
    for (int i = 0; i < 8; ++i) v += *(LAS f32x4*)(L + L_QP + ((i * 8 + (piece >> 1)) * 16 + bb * 8 + (piece & 1) * 4) * 4);
    if (lane < 32) __builtin_amdgcn_raw_buffer_store_b128(ntag4(v, tlo), rs, WS_MQ + (kvh * 2 + bb) * 8192 + kh * 4096 + cbr * 2048 + cg * 256 + piece * 16, 0, AUX_SC1);
  }
  STAMP(2);
  {
    const int skw = lane < 32 ? (lane >> 2) : 8 + ((lane >> 2) & 1);
    u32x4 sv;
#pragma nounroll
    for (u32 spins = 0; spins < SPIN_CAP; ++spins) {
      sv = *(volatile LAS u32x4*)(L + L_ST + (skw * 4 + (lane & 3)) * 16);
      if (__all((sv[1] == tlo) & (sv[3] == thi))) break;
      __builtin_amdgcn_s_sleep(4);
    }
    CBAR();
    STAMP(4);
    const float mw = __uint_as_float(sv[0]);
    float M = mw;
    M = fmaxf(M, dpp<ROR4>(M)); M = fmaxf(M, dpp<ROR8>(M)); M = s16_max(M);
    const float beta = __builtin_amdgcn_exp2f(mw - M);
    float Ls = (lane >= 40) ? 0.f : __uint_as_float(sv[2]) * beta;
    Ls += dpp<ROR4>(Ls); Ls += dpp<ROR8>(Ls); Ls = s16_sum(Ls);
    if (lane < 40) *(volatile LAS u32x2*)(L + L_BETA + lane * 8) = u32x2{__float_as_uint(beta), thi};
    if ((lane & 28) == 0)
      __builtin_amdgcn_raw_buffer_store_b128(u32x4{__float_as_uint(M), tlo, __float_as_uint(Ls), thi}, rs, WS_MS + (kvh * 32 + jj) * 128 + ((lane >> 5) * 4 + (lane & 3)) * 16, 0, AUX_SC1);
  }
  BAR();
  {
    const int moff = lane < 32 ? WS_MS + (kvh * 32 + lane) * 128 + (cbr * 4 + hh) * 16 : WS_BYTES;
    u32x4 m;
#pragma nounroll
    for (u32 spins = 0; spins < SPIN_CAP; ++spins) {
      CBAR();
      m = __builtin_amdgcn_raw_buffer_load_b128(rs, moff, 0, AUX_SC1);
      if ((u32)__ballot(((m[1] ^ tlo) | (m[3] ^ thi)) != 0) == 0) break;
    }
    const float ms = __uint_as_float(m[0]);
    float Mg = ms;
    Mg = fmaxf(Mg, dpp<XOR1>(Mg)); Mg = fmaxf(Mg, dpp<XOR2>(Mg)); Mg = fmaxf(Mg, dpp<ROR4>(Mg)); Mg = fmaxf(Mg, dpp<ROR8>(Mg));
    const float al = __builtin_amdgcn_exp2f(ms - Mg);
    float Lg = __uint_as_float(m[2]) * al;
    Lg += dpp<XOR1>(Lg); Lg += dpp<XOR2>(Lg); Lg += dpp<ROR4>(Lg); Lg += dpp<ROR8>(Lg);
    const float wgt = al / Lg;
    if (lane < 32) *(volatile LAS u32*)(L + L_WT + (lane & 7) * 16 + (lane >> 3) * 4) = btag(wgt, (tlo >> ((lane >> 3) * 8)) & 255u);
  }
  STAMP(16);
  {
    const int fbr = (blk >> 2) & 1, fcq = blk & 3, fb = blk >> 7, fe = (blk >> 3) & 15;
    const int pi = lane >> 1;
    int off = WS_H2 + ((pi & 7) + 8 * (16 * fbr + 4 * (pi >> 3) + fcq)) * H2_STR + fb * 512 + fe * 32 + (lane & 1) * 16;
    u32 ooff = (fb * 1024 + fbr * 512 + fcq * 128 + 32 * (fe >> 2) + 16 * (lane >> 5) + 4 * (fe & 3) + 2 * (lane & 1)) * 2;
    asm volatile("" : "+v"(off), "+v"(ooff));
    BAR();
    __builtin_amdgcn_s_sleep(H2_DELAY);
    u32x4 a = {1u, 1u, 1u, 1u};
    repoll1(a, off, rsv, tlo);
    float x[2];
#pragma unroll
    for (int e2 = 0; e2 < 2; ++e2) {
      auto t = __builtin_amdgcn_permlane32_swap(a[2 * e2], a[2 * e2 + 1], false, false);
      float v = __uint_as_float(t[0]) + __uint_as_float(t[1]);
      v = s16_sum(v); v += dpp<ROR8>(v); v += dpp<ROR4>(v); v += dpp<XOR2>(v);
      x[e2] = v;
    }
    if ((lane & 30) == 0) *(GAS u32*)((GAS unsigned char*)p.out + ooff) = cvtpk(x[0], x[1]);
    if (blk == 0 && lane == 0) __builtin_amdgcn_raw_buffer_store_b64(u32x2{tlo, thi}, rs, WS_E, 0, 0);
  }
  STAMP(13);
}

extern "C" void kernel_launch(void* const* d_in, const int* in_sizes, int n_in, void* d_out, int out_size, void* d_ws, size_t ws_size, hipStream_t stream) {
  Params p;
  for (int br = 0; br < 2; ++br) {
    p.x[br] = (const u16*)d_in[br * 5 + 0]; p.k[br] = (const u16*)d_in[br * 5 + 1]; p.v[br] = (const u16*)d_in[br * 5 + 2];
    p.wq[br] = (const u16*)d_in[br * 5 + 3]; p.wo[br] = (const u16*)d_in[br * 5 + 4];
  }
  p.out = (u16*)d_out; p.ws = (unsigned char*)d_ws;
  fused_decode<<<256, 768, 0, stream>>>(p);
}
```

```cpp
#include <hip/hip_runtime.h>
#include <stdint.h>

typedef unsigned short u16;
typedef unsigned int u32;
typedef unsigned long long u64;
typedef u32 u32x2 __attribute__((ext_vector_type(2)));
typedef u32 u32x4 __attribute__((ext_vector_type(4)));
typedef float f32x2 __attribute__((ext_vector_type(2)));
typedef float f32x4 __attribute__((ext_vector_type(4)));
typedef short s16x4 __attribute__((ext_vector_type(4)));
typedef short s16x8 __attribute__((ext_vector_type(8)));
typedef __bf16 bf16x2 __attribute__((ext_vector_type(2)));
#define GAS __attribute__((address_space(1)))
#define LAS __attribute__((address_space(3)))

#define PACE_0 80
#define PACE_K 32
#define PACE_V 34
#define GRADE 40
#define Q_TRIG 12
#define WO_EARLY 14
#define GO_C 65
#define H2_DELAY 20

struct Params {
  const u16* x[2]; const u16* k[2]; const u16* v[2]; const u16* wq[2]; const u16* wo[2];
  u16* out; unsigned char* ws;
};
static_assert(sizeof(Params) == 96 && __builtin_offsetof(Params, k) == 0x10 && __builtin_offsetof(Params, v) == 0x20 && __builtin_offsetof(Params, wq) == 0x30 &&
              __builtin_offsetof(Params, wo) == 0x40 && __builtin_offsetof(Params, out) == 0x50 && __builtin_offsetof(Params, ws) == 0x58, "the kernel reads its arguments by offset");

constexpr int WS_E = 0, WS_G = 1024, WS_MS = 1 << 15, WS_MQ = 1 << 16, WS_H2 = 3 << 16, WS_H1 = 1 << 19, WS_BYTES = 2 << 20;
constexpr int H1_STR = 4096 + 128, H2_STR = 1024 + 128;
constexpr int KV_BYTES = 2 * 8 * 8192 * 128 * 2, W_BYTES = 512 * 4096 * 2;
constexpr int AUX_SC1 = 16, AUX_NT = 2;
constexpr u32 SPIN_CAP = 1u << 20, LDS_CAP = 1u << 23;

__device__ __forceinline__ f32x4 mfma16(u32x4 a, u32x4 b, f32x4 c) {
  return __builtin_amdgcn_mfma_f32_16x16x32_bf16(__builtin_bit_cast(s16x8, a), __builtin_bit_cast(s16x8, b), c, 0, 0, 0);
}
__device__ __forceinline__ u32 cvtpk(float lo, float hi) {
  f32x2 v = {lo, hi};
  return __builtin_bit_cast(u32, __builtin_convertvector(v, bf16x2));
}
__device__ __forceinline__ float lo_f(u32 v) { return __builtin_bit_cast(float, v << 16); }
__device__ __forceinline__ float hi_f(u32 v) { return __builtin_bit_cast(float, v & 0xffff0000u); }
__device__ __forceinline__ float dot2(u32 a, u32 b, float acc) {
  return __builtin_amdgcn_fdot2_f32_bf16(__builtin_bit_cast(bf16x2, a), __builtin_bit_cast(bf16x2, b), acc, false);
}
template <int CTRL> __device__ __forceinline__ float dpp(float x) {
  return __builtin_bit_cast(float, __builtin_amdgcn_mov_dpp(__builtin_bit_cast(int, x), CTRL, 0xf, 0xf, true));
}
constexpr int XOR1 = 0xB1, XOR2 = 0x4E, ROR4 = 0x124, ROR8 = 0x128;
__device__ __forceinline__ float s16_max(float x) {
  auto s = __builtin_amdgcn_permlane16_swap(__float_as_uint(x), __float_as_uint(x), false, false);
  return fmaxf(__uint_as_float(s[0]), __uint_as_float(s[1]));
}
__device__ __forceinline__ float s32_max(float x) {
  auto t = __builtin_amdgcn_permlane32_swap(__float_as_uint(x), __float_as_uint(x), false, false);
  return fmaxf(__uint_as_float(t[0]), __uint_as_float(t[1]));
}
__device__ __forceinline__ float s16_sum(float x) {
  auto s = __builtin_amdgcn_permlane16_swap(__float_as_uint(x), __float_as_uint(x), false, false);
  return __uint_as_float(s[0]) + __uint_as_float(s[1]);
}
__device__ __forceinline__ float s32_sum(float x) {
  auto t = __builtin_amdgcn_permlane32_swap(__float_as_uint(x), __float_as_uint(x), false, false);
  return __uint_as_float(t[0]) + __uint_as_float(t[1]);
}
__device__ __forceinline__ u32 btag(float v, u32 tb) { return ((__float_as_uint(v) + 0x80u) & 0xFFFFFF00u) | tb; }
__device__ __forceinline__ u32x4 btag4(f32x4 v, u32 t) { u32x4 r = {btag(v[0], t & 255u), btag(v[1], (t >> 8) & 255u), btag(v[2], (t >> 16) & 255u), btag(v[3], t >> 24)}; return r; }
__device__ __forceinline__ u32x4 untag4(u32x4 v, u32 t) { u32x4 r = {v[0] ^ (t & 255u), v[1] ^ ((t >> 8) & 255u), v[2] ^ ((t >> 16) & 255u), v[3] ^ (t >> 24)}; return r; }
__device__ __forceinline__ u32 or4(u32x4 v) { return v[0] | v[1] | v[2] | v[3]; }
__device__ __forceinline__ u32x4 ntag4(f32x4 v, u32 t) {
  u32x4 r;
#pragma unroll
  for (int i = 0; i < 4; ++i) r[i] = ((__float_as_uint(v[i]) + 8u) & ~15u) | ((t >> (8 * i)) & 15u);
  return r;
}
__device__ __forceinline__ u64 mix64(u64 z) {
  z += 0x9E3779B97F4A7C15ull; z = (z ^ (z >> 30)) * 0xBF58476D1CE4E5B9ull; z = (z ^ (z >> 27)) * 0x94D049BB133111EBull; return z ^ (z >> 31);
}
#define BAR() do { __builtin_amdgcn_sched_barrier(0); asm volatile("s_waitcnt lgkmcnt(0)" ::: "memory"); __builtin_amdgcn_s_barrier(); asm volatile("" ::: "memory"); __builtin_amdgcn_sched_barrier(0); } while (0)
#define VMCNT(n) asm volatile("s_waitcnt vmcnt(%0)" :: "i"(n) : "memory")
#define LGKM0() asm volatile("s_waitcnt lgkmcnt(0)" ::: "memory")
#define CBAR() asm volatile("" ::: "memory")
#define SB() __builtin_amdgcn_sched_barrier(0)

#define RP_MASK(U, M) \
  "v_or3_b32 v144, %[" U "0], %[" U "1], %[" U "2]\n\tv_bitop3_b32 v144, v144, s73, %[" U "3] bitop3:0xc8\n\tv_cmp_ne_u32 vcc, 0, v144\n\ts_wqm_b64 " M ", vcc\n\t"
#define RP_LOAD(M, R, O, L) \
  "s_mov_b64 exec, " M "\n\ts_cbranch_execz " L "f\n\tbuffer_load_dwordx4 " R ", %[" O "], %[rs], 0 offen sc1\n" L ":\n\t"
#define RP_TAKE(U, M, R0, R1, R2, R3) \
  "s_mov_b64 exec, " M "\n\tv_xor_b32 %[" U "0], %[b0], " R0 "\n\tv_xor_b32 %[" U "1], %[b1], " R1 "\n\tv_xor_b32 %[" U "2], %[b2], " R2 "\n\tv_xor_b32 %[" U "3], %[b3], " R3 "\n\t"
__device__ __forceinline__ void repoll4(u32x4& a, u32x4& b, u32x4& c, u32x4& d, int oa, int ob, int oc, int od, u32x4 rsrc, u32 tlo) {
  u32 a0 = a[0], a1 = a[1], a2 = a[2], a3 = a[3], b0 = b[0], b1 = b[1], b2 = b[2], b3 = b[3];
  u32 c0 = c[0], c1 = c[1], c2 = c[2], c3 = c[3], d0 = d[0], d1 = d[1], d2 = d[2], d3 = d[3];
  asm volatile(
    "s_mov_b32 s72, 0x100000\n\t"
    "s_movk_i32 s73, 0xff\n"
    "1:\n\t"
    RP_MASK("p", "s[64:65]") RP_MASK("q", "s[66:67]") RP_MASK("r", "s[68:69]") RP_MASK("t", "s[70:71]")
    "s_or_b64 vcc, s[64:65], s[66:67]\n\t"
    "s_or_b64 vcc, vcc, s[68:69]\n\t"
    "s_or_b64 vcc, vcc, s[70:71]\n\t"
    "s_cbranch_scc0 2f\n\t"
    RP_LOAD("s[64:65]", "v[128:131]", "oa", "10") RP_LOAD("s[66:67]", "v[132:135]", "ob", "11")
    RP_LOAD("s[68:69]", "v[136:139]", "oc", "12") RP_LOAD("s[70:71]", "v[140:143]", "od", "13")
    "s_waitcnt vmcnt(0)\n\t"
    RP_TAKE("p", "s[64:65]", "v128", "v129", "v130", "v131") RP_TAKE("q", "s[66:67]", "v132", "v133", "v134", "v135")
    RP_TAKE("r", "s[68:69]", "v136", "v137", "v138", "v139") RP_TAKE("t", "s[70:71]", "v140", "v141", "v142", "v143")
    "s_mov_b64 exec, -1\n\t"
    "s_sub_u32 s72, s72, 1\n\t"
    "s_cbranch_scc0 1b\n"
    "2:"
    : [p0] "+v"(a0), [p1] "+v"(a1), [p2] "+v"(a2), [p3] "+v"(a3), [q0] "+v"(b0), [q1] "+v"(b1), [q2] "+v"(b2), [q3] "+v"(b3),
      [r0] "+v"(c0), [r1] "+v"(c1), [r2] "+v"(c2), [r3] "+v"(c3), [t0] "+v"(d0), [t1] "+v"(d1), [t2] "+v"(d2), [t3] "+v"(d3)
    : [oa] "v"(oa), [ob] "v"(ob), [oc] "v"(oc), [od] "v"(od), [rs] "s"(rsrc),
      [b0] "s"(tlo & 255u), [b1] "s"((tlo >> 8) & 255u), [b2] "s"((tlo >> 16) & 255u), [b3] "s"(tlo >> 24)
    : "memory", "vcc", "scc", "v128", "v129", "v130", "v131", "v132", "v133", "v134", "v135", "v136", "v137", "v138", "v139", "v140", "v141", "v142", "v143", "v144",
      "s64", "s65", "s66", "s67", "s68", "s69", "s70", "s71", "s72", "s73");
  a = u32x4{a0, a1, a2, a3}; b = u32x4{b0, b1, b2, b3}; c = u32x4{c0, c1, c2, c3}; d = u32x4{d0, d1, d2, d3};
}
__device__ __forceinline__ void repoll1(u32x4& a, int oa, u32x4 rsrc, u32 tlo) {
  u32 a0 = a[0], a1 = a[1], a2 = a[2], a3 = a[3];
  asm volatile(
    "s_mov_b32 s72, 0x100000\n\t"
    "s_movk_i32 s73, 0xff\n"
    "1:\n\t"
    RP_MASK("p", "exec")
    "s_cbranch_scc0 2f\n\t"
    "buffer_load_dwordx4 v[128:131], %[oa], %[rs], 0 offen sc1\n\t"
    "s_sub_u32 s72, s72, 1\n\t"
    "s_waitcnt vmcnt(0)\n\t"
    "v_xor_b32 %[p0], %[b0], v128\n\tv_xor_b32 %[p1], %[b1], v129\n\tv_xor_b32 %[p2], %[b2], v130\n\tv_xor_b32 %[p3], %[b3], v131\n\t"
    "s_cbranch_scc0 1b\n"
    "2:\n\t"
    "s_mov_b64 exec, -1"
    : [p0] "+v"(a0), [p1] "+v"(a1), [p2] "+v"(a2), [p3] "+v"(a3)
    : [oa] "v"(oa), [rs] "s"(rsrc), [b0] "s"(tlo & 255u), [b1] "s"((tlo >> 8) & 255u), [b2] "s"((tlo >> 16) & 255u), [b3] "s"(tlo >> 24)
    : "memory", "vcc", "scc", "v128", "v129", "v130", "v131", "v144", "s72", "s73");
  a = u32x4{a0, a1, a2, a3};
}

constexpr int L_WO = 0;
constexpr int L_RED = 32768;
constexpr int L_SQ = L_RED + 20480;
constexpr int L_QP = L_SQ + 2048;
constexpr int L_O2 = L_QP + 4096;
constexpr int L_T0 = L_O2 + 512;
constexpr int L_SYNC = L_T0 + 16;
constexpr int L_ST = L_SYNC;
constexpr int L_BETA = L_ST + 640;
constexpr int L_WT = L_BETA + 320;
constexpr int L_FLAG = L_WT + 128;
constexpr int F_QP = 0, F_Q = 8, F_QTRIG = 16, F_WO = 24, F_VD = 32;
constexpr int L_BYTES = L_FLAG + F_VD + 80;
static_assert(L_BYTES - L_SYNC <= 1280 && (L_BYTES - L_SYNC) % 16 == 0 && L_SYNC % 16 == 0, "cleared by two stores per lane");

template <int SLEEP> __device__ __forceinline__ void lds_wait_flag(LAS unsigned char* L, int off, u64 tag) {
#pragma nounroll
  for (u32 spins = 0; spins < LDS_CAP; ++spins) {
    if (*(volatile LAS u64*)(L + L_FLAG + off) == tag) break;
    if (SLEEP) __builtin_amdgcn_s_sleep(SLEEP);
  }
  CBAR();
}
#define SET_FLAG(off) do { if (lane == 0) *(volatile LAS u64*)(L + L_FLAG + (off)) = tag; } while (0)

#define MAKE_TAG() \
  SB(); \
  u64 E; asm volatile("s_load_dwordx2 %0, %1, 0x0\n\ts_waitcnt lgkmcnt(0)" : "=s"(E) : "s"(ws) : "memory"); \
  const u64 mixed = mix64(E); \
  const u64 tx_ = mixed ^ E; \
  const u64 tag = mixed ^ ((tx_ - 0x1111111111111111ull) & ~tx_ & 0x8888888888888888ull); \
  const u32 tlo = (u32)tag, thi = (u32)(tag >> 32); \
  SB();

__global__ void __launch_bounds__(768, 3) fused_decode(Params) {
  __shared__ __attribute__((aligned(16))) unsigned char smem[L_BYTES];
  LAS unsigned char* L = (LAS unsigned char*)smem;
  const int blk = blockIdx.x, tid = threadIdx.x, lane = tid & 63;
  const int wave = __builtin_amdgcn_readfirstlane(tid >> 6);
  const int kvh = blk & 7, jj = blk >> 3;
  const int g = lane >> 4, r = lane & 15;
  const u64 ka = (u64)__builtin_amdgcn_kernarg_segment_ptr();
#define WS_RSRC() \
  const __amdgpu_buffer_rsrc_t rs = __builtin_amdgcn_make_buffer_rsrc(ws, 0, WS_BYTES, 0x00020000); \
  const u32x4 rsv = {(u32)(u64)ws, (u32)((u64)ws >> 32) & 0xffffu, (u32)WS_BYTES, 0x00020000u};
  constexpr float LOG2E = 1.4426950408889634f;
  const int cbr = jj >> 4, hh = (jj >> 2) & 3;

  if (wave >= 2) {
    const int kw = wave - 2, br = kw >= 8 ? 1 : 0;
    const int key0 = br ? 6144 + 128 * (jj & 15) + 64 * (kw - 8) : 512 * (jj & 15) + 64 * kw;
    u32 base = (((jj >> 4) * 8 + kvh) * 8192 + key0) * 128;
#define PAR(m) (__builtin_popcount((base >> 13) & (m)) & 1)
    int rot = (PAR(0x08a) << 11) | (PAR(0x500) << 12) | (PAR(0x0e9) << 13);
    u32 kl = (base + (r >> 2) * 128 + (4 * g + (r & 3)) * 8) * 2, vl = (base + lane * 8) * 2;
    asm volatile("" : "+v"(kl), "+v"(vl), "+s"(rot));
    const unsigned char *kp, *vp; unsigned char* ws;
    asm volatile("s_load_dwordx2 %0, %3, %4 offset:0x10\n\ts_load_dwordx2 %1, %3, %4 offset:0x20\n\ts_load_dwordx2 %2, %3, 0x58\n\ts_waitcnt lgkmcnt(0)"
                 : "=&s"(kp), "=&s"(vp), "=&s"(ws) : "s"(ka), "s"(br * 8));
    WS_RSRC();
    const __amdgpu_buffer_rsrc_t rk = __builtin_amdgcn_make_buffer_rsrc((void*)kp, 0, KV_BYTES, 0x00020000);
    const __amdgpu_buffer_rsrc_t rv = __builtin_amdgcn_make_buffer_rsrc((void*)vp, 0, KV_BYTES, 0x00020000);
    u32x4 Kf[16], Vf[16];
#define KLOAD(n) do { SB(); Kf[n] = __builtin_amdgcn_raw_buffer_load_b128(rk, kl, ((n) * 1024) ^ rot, AUX_NT); SB(); } while (0)
#define VLOAD(n) do { SB(); Vf[n] = __builtin_amdgcn_raw_buffer_load_b128(rv, vl, ((n) * 1024) ^ rot, AUX_NT); SB(); } while (0)
#define THROTTLE(due) do { VMCNT(5); const u32 t_ = (u32)__builtin_amdgcn_s_memrealtime() - pt0; \
    if (t_ < (u32)((due) + 3 * GRADE)) { VMCNT(4); if (t_ < (u32)((due) + 2 * GRADE)) { VMCNT(3); \
    if (t_ < (u32)((due) + GRADE)) { VMCNT(2); if (t_ < (u32)(due)) VMCNT(1); } } } } while (0)
#define VSTEP(n) do { THROTTLE(PACE_0 + 16 * PACE_K + (n) * PACE_V); VLOAD(n); } while (0)
    KLOAD(0); KLOAD(1);
    MAKE_TAG();
    BAR();
    const u32 pt0 = __builtin_amdgcn_readfirstlane(*(volatile LAS u32*)(L + L_T0));
#pragma unroll
    for (int n = 2; n < 16; ++n) {
      THROTTLE(PACE_0 + n * PACE_K);
      KLOAD(n);
      if (n == Q_TRIG && kw == 0) SET_FLAG(F_QTRIG);
    }
    VSTEP(0); VSTEP(1);
    lds_wait_flag<1>(L, F_Q, tag);
    const u32x4 Qf = *(LAS u32x4*)(L + L_SQ + br * 1024 + (r >> 2) * 256 + (4 * g + (r & 3)) * 16);
    float sc[16];
#pragma unroll
    for (int c = 0; c < 4; ++c) {
      f32x4 d[4];
#pragma unroll
      for (int i = 0; i < 4; ++i) d[i] = mfma16(Kf[4 * c + i], Qf, f32x4{0.f, 0.f, 0.f, 0.f});
#pragma unroll
      for (int i = 0; i < 4; ++i) sc[4 * c + i] = (dpp<0x00>(d[i][0]) + dpp<0x55>(d[i][1])) + (dpp<0xAA>(d[i][2]) + dpp<0xFF>(d[i][3]));
#pragma unroll
      for (int i = 4 * c; i < 4 * c + 4; i += 2) {
        const u32 a0 = cvtpk(sc[i], sc[i + 1]);
        const u32 c0 = cvtpk(lo_f(a0) * 0.08838834764831845f, hi_f(a0) * 0.08838834764831845f);
        sc[i] = lo_f(c0); sc[i + 1] = hi_f(c0);
      }
      VSTEP(2 + c);
    }
    float mx = sc[0];
#pragma unroll
    for (int i = 1; i < 16; ++i) mx = fmaxf(mx, sc[i]);
    mx = s32_max(s16_max(mx));
    const float M2 = mx * LOG2E;
    VSTEP(6);
    float ls = 0.f;
    u32x4 Pf[2];
#pragma unroll
    for (int u = 0; u < 2; ++u) {
#pragma unroll
      for (int i = 8 * u; i < 8 * u + 8; ++i) { sc[i] = __builtin_amdgcn_exp2f(__builtin_fmaf(sc[i], LOG2E, -M2)); ls += sc[i]; }
#pragma unroll
      for (int t = 0; t < 4; ++t) Pf[u][t] = cvtpk(sc[8 * u + 2 * t], sc[8 * u + 2 * t + 1]);
      if (u == 1) {
        ls = s32_sum(s16_sum(ls));
        if ((lane & 51) == 0) *(LAS u32x4*)(L + L_ST + (kw * 4 + (lane >> 2)) * 16) = u32x4{__float_as_uint(M2), tlo, __float_as_uint(ls), thi};
      }
      VSTEP(7 + u);
    }
    VSTEP(9); VSTEP(10); VSTEP(11);
    f32x4 acc[8];
    u32x4 F[8];
#pragma unroll
    for (int j = 0; j < 8; ++j) {
#pragma unroll
      for (int t = 0; t < 4; ++t) F[j][t] = __builtin_amdgcn_perm(Vf[2 * t + 1][j >> 1], Vf[2 * t][j >> 1], (j & 1) ? 0x07060302u : 0x05040100u);
      acc[j] = mfma16(Pf[0], F[j], f32x4{0.f, 0.f, 0.f, 0.f});
      if (j == 3) VSTEP(12);
    }
    VSTEP(13); VSTEP(14); VSTEP(15);
    SET_FLAG(F_VD + kw * 8);
    float beta;
#pragma nounroll
    for (u32 spins = 0; spins < LDS_CAP; ++spins) {
      const u32x2 bv = *(volatile LAS u32x2*)(L + L_BETA + (kw * 4 + g) * 8);
      beta = __uint_as_float(bv[0]);
      if (__all(bv[1] == thi)) break;
      __builtin_amdgcn_s_sleep(1);
    }
    if (kw >= 4 && kw < 8) __builtin_amdgcn_s_setprio(3);
    u32 sumr = (u32)(size_t)(L + L_RED + ((kw >> 1) & 1) * 16384 + (kw & 1) * 1024 + lane * 16);
    int h1off = WS_H1 + blk * H1_STR + ((kw >> 1) & 1) * 2048 + g * 512 + (kw & 1) * 256 + r * 16;
    asm volatile("" : "+v"(sumr), "+v"(h1off));
#pragma unroll
    for (int t = 0; t < 4; ++t) {
#pragma unroll
      for (int j = 0; j < 8; ++j) F[j][t] = __builtin_amdgcn_perm(Vf[8 + 2 * t + 1][j >> 1], Vf[8 + 2 * t][j >> 1], (j & 1) ? 0x07060302u : 0x05040100u);
      if (t < 3) SB();
    }
#pragma unroll
    for (int j = 0; j < 8; ++j) acc[j] = mfma16(Pf[1], F[j], acc[j]);
    *(LAS f32x4*)(L + L_RED + kw * 2048 + lane * 16) = f32x4{acc[0][0], acc[1][0], acc[2][0], acc[3][0]} * beta;
    *(LAS f32x4*)(L + L_RED + kw * 2048 + 1024 + lane * 16) = f32x4{acc[4][0], acc[5][0], acc[6][0], acc[7][0]} * beta;
    BAR();
    if (__builtin_expect(kw >= 4, 1)) {
      if (__builtin_expect(kw < 6, 1)) {
        f32x4 t[8];
#pragma unroll
        for (int w = 0; w < 8; ++w) t[w] = *(LAS f32x4*)(size_t)(sumr + w * 2048);
        SB();
        const f32x4 v = ((t[0] + t[1]) + (t[2] + t[3])) + ((t[4] + t[5]) + (t[6] + t[7]));
        __builtin_amdgcn_raw_buffer_store_b128(btag4(v, tlo), rs, h1off, 0, AUX_SC1);
      } else if (kw < 8) {
        const f32x4 v = *(LAS f32x4*)(size_t)sumr + *(LAS f32x4*)(size_t)(sumr + 2048);
        __builtin_amdgcn_raw_buffer_store_b128(btag4(v, tlo), rs, h1off, 0, AUX_SC1);
      }
      return;
    }
    u32 o2w = (u32)(size_t)(L + L_O2 + (lane >> 5) * 256 + kw * 64 + (lane & 7) * 8 + ((lane >> 4) & 1) * 4);
    u32 o2r = (u32)(size_t)(L + L_O2 + ((r >> 2) & 1) * 256 + g * 16);
    int h2off = lane < 32 ? WS_H2 + blk * H2_STR + g * 512 + kw * 128 + r * 8 : WS_BYTES;
    u32 tb0 = (tlo >> ((r & 1) * 16)) & 255u, tb1 = (tlo >> ((r & 1) * 16 + 8)) & 255u;
    int o0 = WS_H1 + (kvh + 8 * (lane >> 3)) * H1_STR + cbr * 2048 + hh * 512 + kw * 128 + (lane & 7) * 16;
    int o1 = o0 + 64 * H1_STR, o2 = o0 + 128 * H1_STR, o3 = o0 + 192 * H1_STR;
    u32 wr0 = (u32)(size_t)(L + L_WO + (8 * g + (r >> 2)) * 256 + (((4 * kw + ((r & 3) >> 1)) ^ (2 * ((r >> 2) | ((g & 1) << 2)))) * 16) + (r & 1) * 8);
    u32 wr1 = wr0 ^ 32u;
    asm volatile("" : "+v"(o2w), "+v"(o2r), "+v"(h2off), "+v"(tb0), "+v"(tb1), "+v"(o0), "+v"(o1), "+v"(o2), "+v"(o3), "+v"(wr0), "+v"(wr1));
    u32x4 wu;
#pragma nounroll
    for (u32 spins = 0; spins < LDS_CAP; ++spins) {
      wu = untag4(*(volatile LAS u32x4*)(L + L_WT + (lane >> 3) * 16), tlo);
      if (!__any((or4(wu) & 255u) != 0)) break;
      __builtin_amdgcn_s_sleep(1);
    }
    u32x4 u0 = __builtin_amdgcn_raw_buffer_load_b128(rs, o0, 0, AUX_SC1), u1 = __builtin_amdgcn_raw_buffer_load_b128(rs, o1, 0, AUX_SC1);
    u32x4 u2 = __builtin_amdgcn_raw_buffer_load_b128(rs, o2, 0, AUX_SC1), u3 = __builtin_amdgcn_raw_buffer_load_b128(rs, o3, 0, AUX_SC1);
    SB();
    lds_wait_flag<0>(L, F_WO, tag);
    u32x4 wf[8];
#pragma unroll
    for (int i = 0; i < 8; ++i) {
      const u32 a = ((i & 1) ? wr1 : wr0) + (i >> 1) * 8192;
      const u32x2 h0 = __builtin_bit_cast(u32x2, __builtin_amdgcn_ds_read_tr16_b64_v4i16((LAS s16x4*)(size_t)a));
      const u32x2 h1 = __builtin_bit_cast(u32x2, __builtin_amdgcn_ds_read_tr16_b64_v4i16((LAS s16x4*)(size_t)(a + 1024)));
      wf[i] = u32x4{h0[0], h0[1], h1[0], h1[1]};
    }
    SB();
    u0 = untag4(u0, tlo); u1 = untag4(u1, tlo); u2 = untag4(u2, tlo); u3 = untag4(u3, tlo);
    if (__any(((or4(u0) | or4(u1) | or4(u2) | or4(u3)) & 255u) != 0)) repoll4(u0, u1, u2, u3, o0, o1, o2, o3, rsv, tlo);
    {
      float x[4];
#pragma unroll
      for (int e = 0; e < 4; ++e) {
        float s0 = __uint_as_float(u0[e]) * __uint_as_float(wu[0]), s1 = __uint_as_float(u2[e]) * __uint_as_float(wu[2]);
        asm("" : "+v"(s0)); asm("" : "+v"(s1));
        s0 = __builtin_fmaf(__uint_as_float(u1[e]), __uint_as_float(wu[1]), s0); s1 = __builtin_fmaf(__uint_as_float(u3[e]), __uint_as_float(wu[3]), s1);
        asm("" : "+v"(s0)); asm("" : "+v"(s1));
        auto t = __builtin_amdgcn_permlane32_swap(__float_as_uint(s0), __float_as_uint(s1), false, false);
        x[e] = __uint_as_float(t[0]) + __uint_as_float(t[1]);
      }
      float y[2];
#pragma unroll
      for (int e = 0; e < 2; ++e) {
        auto t = __builtin_amdgcn_permlane16_swap(__float_as_uint(x[e]), __float_as_uint(x[e + 2]), false, false);
        y[e] = __uint_as_float(t[0]) + __uint_as_float(t[1]);
        asm("" : "+v"(y[e]));
        y[e] += dpp<ROR8>(y[e]);
      }
      *(LAS u32*)(size_t)o2w = cvtpk(y[0], y[1]);
    }
    BAR();
    {
      u32x4 Of[4];
#pragma unroll
      for (int qd = 0; qd < 4; ++qd) Of[qd] = *(LAS u32x4*)(size_t)(o2r + qd * 64);
      SB();
      f32x4 d0 = {0.f, 0.f, 0.f, 0.f}, d1 = {0.f, 0.f, 0.f, 0.f};
#pragma unroll
      for (int qd = 0; qd < 4; ++qd) {
        d0 = mfma16(Of[qd], wf[2 * qd], d0);
        d1 = mfma16(Of[qd], wf[2 * qd + 1], d1);
      }
      __builtin_amdgcn_raw_buffer_store_b64(u32x2{btag(d0[0], tb0), btag(d1[0], tb1)}, rs, h2off, 0, AUX_SC1);
    }
    asm volatile(".Lwarm_end:");
    return;
  }

  u32 cw0 = 0, cw1 = 0;
  if (wave == 0 && jj < 2) {
    u64 pc0; u32 len;
    asm volatile("s_getpc_b64 %0\n.Lwarm_pc:\n\ts_mov_b32 %1, .Lwarm_end-.Lwarm_pc+512" : "=s"(pc0), "=s"(len));
    const __amdgpu_buffer_rsrc_t cr = __builtin_amdgcn_make_buffer_rsrc((void*)pc0, 0, len, 0x00020000);
    cw0 = __builtin_amdgcn_raw_buffer_load_b32(cr, lane * 128, 0, 0); cw1 = __builtin_amdgcn_raw_buffer_load_b32(cr, 8192 + lane * 128, 0, 0);
  }
  SB();
  const int hw = wave;
  const int kh = jj & 1, cg = (jj >> 1) & 7;
  u32 xl = (256 * kh + 128 * hw + 16 * (lane >> 3)) * 2, wl = xl * 4096 + (kvh * 512 + 64 * cg + 8 * (lane & 7)) * 2;
  asm volatile("" : "+v"(xl), "+v"(wl));
  const unsigned char *xp0, *wqp; const u16* wop0; u16* outp; unsigned char* ws;
  asm volatile("s_load_dwordx2 %0, %5, %6 offset:0x0\n\ts_load_dwordx2 %1, %5, %6 offset:0x30\n\ts_load_dwordx2 %2, %5, %6 offset:0x40\n\t"
               "s_load_dwordx2 %3, %5, 0x50\n\ts_load_dwordx2 %4, %5, 0x58\n\ts_waitcnt lgkmcnt(0)"
               : "=&s"(xp0), "=&s"(wqp), "=&s"(wop0), "=&s"(outp), "=&s"(ws) : "s"(ka), "s"(cbr * 8));
  WS_RSRC();
  __builtin_amdgcn_s_setprio(3);
  const __amdgpu_buffer_rsrc_t rq = __builtin_amdgcn_make_buffer_rsrc((void*)wqp, 0, W_BYTES, 0x00020000);
  u32x4 xv[2][2], w[16];
#pragma unroll
  for (int i = 0; i < 4; ++i) xv[i >> 1][i & 1] = *(const GAS u32x4*)(xp0 + (i >> 1) * 1024 + (i & 1) * 16 + xl);
  SB();
#pragma unroll
  for (int i = 0; i < 16; ++i) { w[i] = __builtin_amdgcn_raw_buffer_load_b128(rq, wl, i * 8192, AUX_NT); SB(); }
  MAKE_TAG();
  if (hw == 0) {
    const u32x4 nt4 = {~tlo, ~thi, ~tlo, ~thi};
    *(LAS u32x4*)(L + L_SYNC + lane * 16) = nt4;
    if (lane < (L_BYTES - L_SYNC - 1024) / 16) *(LAS u32x4*)(L + L_SYNC + 1024 + lane * 16) = nt4;
  } else {
    asm volatile("" :: "v"(xv[1][1]));
    if (lane == 0) *(volatile LAS u32*)(L + L_T0) = (u32)__builtin_amdgcn_s_memrealtime();
  }
  BAR();
  {
    float qa[2][8];
#pragma unroll
    for (int bb = 0; bb < 2; ++bb)
#pragma unroll
      for (int j = 0; j < 8; ++j) qa[bb][j] = 0.f;
#pragma unroll
    for (int t = 0; t < 8; ++t)
#pragma unroll
      for (int j = 0; j < 8; ++j) {
        const u32 pr = __builtin_amdgcn_perm(w[2 * t + 1][j >> 1], w[2 * t][j >> 1], (j & 1) ? 0x07060302u : 0x05040100u);
#pragma unroll
        for (int bb = 0; bb < 2; ++bb) qa[bb][j] = dot2(pr, xv[bb][t >> 2][t & 3], qa[bb][j]);
      }
#pragma unroll
    for (int bb = 0; bb < 2; ++bb)
#pragma unroll
      for (int j = 0; j < 8; ++j) qa[bb][j] += dpp<ROR8>(qa[bb][j]);
    if (r < 8) {
#pragma unroll
      for (int bb = 0; bb < 2; ++bb)
#pragma unroll
        for (int h = 0; h < 2; ++h)
          *(LAS f32x4*)(L + L_QP + (((hw * 4 + g) * 8 + r) * 16 + bb * 8 + h * 4) * 4) = f32x4{qa[bb][4 * h], qa[bb][4 * h + 1], qa[bb][4 * h + 2], qa[bb][4 * h + 3]};
    }
  }
  LGKM0();
  asm volatile("" :: "v"(cw0), "v"(cw1));
  const u32 tq = (((lane & 2) ? thi : tlo) >> ((lane & 1) * 4)) & 0x0F0F0F0Fu;
  if (hw == 1) {
    SET_FLAG(F_QP);
    lds_wait_flag<4>(L, F_QTRIG, tag);
    const int qoff = WS_MQ + (kvh * 2 + (jj >> 4)) * 8192 + lane * 16;
    u32x4 m[8];
#pragma nounroll
    for (u32 spins = 0; spins < SPIN_CAP; ++spins) {
      CBAR();
      u32 bad = 0;
#pragma unroll
      for (int i = 0; i < 8; ++i) m[i] = __builtin_amdgcn_raw_buffer_load_b128(rs, qoff + i * 1024, 0, AUX_SC1);
#pragma unroll
      for (int i = 0; i < 8; ++i) { m[i] = untag4(m[i], tq); bad |= or4(m[i]); }
      if (!__any((bad & 15u) != 0)) break;
      __builtin_amdgcn_s_sleep(16);
    }
    CBAR();
#pragma unroll
    for (int i = 0; i < 4; ++i) {
      const f32x4 v = __builtin_bit_cast(f32x4, m[i]) + __builtin_bit_cast(f32x4, m[i + 4]);
      *(LAS u32x2*)(L + L_SQ + i * 512 + lane * 8) = u32x2{cvtpk(v[0], v[1]), cvtpk(v[2], v[3])};
    }
    LGKM0();
    SET_FLAG(F_Q);
    if (jj == 0 && lane < 8) __builtin_amdgcn_raw_buffer_store_b128(u32x4{tlo, thi, tlo, thi}, rs, WS_G + kvh * 128 + lane * 16, 0, AUX_SC1);
#pragma nounroll
    for (u32 spins = 0; spins < LDS_CAP; ++spins) {
      if (__all(*(volatile LAS u64*)(L + L_FLAG + F_VD + (r < 10 ? r : 0) * 8) == tag)) break;
      __builtin_amdgcn_s_sleep(2);
    }
    CBAR();
    const u16* wop = wop0 + (size_t)(kvh * 512 + hh * 128 + g) * 512 + (jj & 3) * 128;
#pragma unroll
    for (int i = 0; i < 32; ++i) {
      const int dim = 64 * ((i >> 3) & 1) + 8 * (i & 7) + 4 * (i >> 4);
      const int chunk = r ^ (2 * (g | (((i >> 1) & 1) << 2)));
      if (i == WO_EARLY) { __builtin_amdgcn_s_barrier(); __builtin_amdgcn_s_sleep(6); }
      __builtin_amdgcn_global_load_lds((const GAS void*)(wop + dim * 512 + chunk * 8), (LAS void*)(L + L_WO + i * 1024), 16, 0, AUX_NT);
    }
    VMCNT(0);
    SET_FLAG(F_WO);
    LGKM0();
    return;
  }
  lds_wait_flag<1>(L, F_QP, tag);
  {
    const int piece = lane & 15, bb = (lane >> 4) & 1;
    f32x4 v = {0.f, 0.f, 0.f, 0.f};
#pragma unroll
    for (int i = 0; i < 8; ++i) v += *(LAS f32x4*)(L + L_QP + ((i * 8 + (piece >> 1)) * 16 + bb * 8 + (piece & 1) * 4) * 4);
    if (lane < 32) __builtin_amdgcn_raw_buffer_store_b128(ntag4(v, tq), rs, WS_MQ + (kvh * 2 + bb) * 8192 + kh * 4096 + cbr * 2048 + cg * 256 + piece * 16, 0, AUX_SC1);
  }
  u32 ts0;
  {
    const int skw = lane < 32 ? (lane >> 2) : 8 + ((lane >> 2) & 1);
    u32x4 sv;
#pragma nounroll
    for (u32 spins = 0; spins < LDS_CAP; ++spins) {
      sv = *(volatile LAS u32x4*)(L + L_ST + (skw * 4 + (lane & 3)) * 16);
      if (__all((sv[1] == tlo) & (sv[3] == thi))) break;
      __builtin_amdgcn_s_sleep(4);
    }
    CBAR();
    ts0 = (u32)__builtin_amdgcn_s_memrealtime();
    const float mw = __uint_as_float(sv[0]);
    float M = mw;
    M = fmaxf(M, dpp<ROR4>(M)); M = fmaxf(M, dpp<ROR8>(M)); M = s16_max(M);
    const float beta = __builtin_amdgcn_exp2f(mw - M);
    float Ls = (lane >= 40) ? 0.f : __uint_as_float(sv[2]) * beta;
    Ls += dpp<ROR4>(Ls); Ls += dpp<ROR8>(Ls); Ls = s16_sum(Ls);
    if (lane < 40) *(volatile LAS u32x2*)(L + L_BETA + lane * 8) = u32x2{__float_as_uint(beta), thi};
    if ((lane & 28) == 0)
      __builtin_amdgcn_raw_buffer_store_b128(u32x4{__float_as_uint(M), (ts0 << 16) | (tlo & 0xFFFFu), __float_as_uint(Ls), thi}, rs, WS_MS + (kvh * 32 + jj) * 128 + ((lane >> 5) * 4 + (lane & 3)) * 16, 0, AUX_SC1);
  }
  if (blk == 0) {
#pragma nounroll
    for (u32 spins = 0; spins < SPIN_CAP; ++spins) {
      CBAR();
      const u32x4 m = __builtin_amdgcn_raw_buffer_load_b128(rs, WS_G + lane * 16, 0, AUX_SC1);
      if (!__any(((m[0] ^ tlo) | (m[1] ^ thi) | (m[2] ^ tlo) | (m[3] ^ thi)) != 0)) break;
      __builtin_amdgcn_s_sleep(8);
    }
    if (lane < 8) __builtin_amdgcn_raw_buffer_store_b128(u32x4{tlo, thi, tlo, thi}, rs, WS_E + lane * 16, 0, AUX_SC1);
  }
  {
    const int moff = lane < 32 ? WS_MS + (kvh * 32 + lane) * 128 + (cbr * 4 + hh) * 16 : WS_BYTES;
    lds_wait_flag<2>(L, F_VD, tag);
    u32x4 m = __builtin_amdgcn_raw_buffer_load_b128(rs, moff, 0, AUX_SC1);
    SB();
    BAR();
    const u32 tb1 = (u32)__builtin_amdgcn_s_memrealtime();
#pragma nounroll
    for (u32 spins = 0; spins < SPIN_CAP; ++spins) {
      if ((u32)__ballot((((m[1] ^ tlo) << 16) | (m[3] ^ thi)) != 0) == 0) break;
      CBAR();
      m = __builtin_amdgcn_raw_buffer_load_b128(rs, moff, 0, AUX_SC1);
    }
    const float ms = __uint_as_float(m[0]);
    float Mg = ms;
    Mg = fmaxf(Mg, dpp<XOR1>(Mg)); Mg = fmaxf(Mg, dpp<XOR2>(Mg)); Mg = fmaxf(Mg, dpp<ROR4>(Mg)); Mg = fmaxf(Mg, dpp<ROR8>(Mg));
    const float al = __builtin_amdgcn_exp2f(ms - Mg);
    float Lg = __uint_as_float(m[2]) * al;
    Lg += dpp<XOR1>(Lg); Lg += dpp<XOR2>(Lg); Lg += dpp<ROR4>(Lg); Lg += dpp<ROR8>(Lg);
    const float wgt = al / Lg;
    {
      float lag = (float)((int)(m[1] - (ts0 << 16)) >> 16);
      lag = fmaxf(lag, dpp<XOR1>(lag)); lag = fmaxf(lag, dpp<XOR2>(lag)); lag = fmaxf(lag, dpp<ROR4>(lag)); lag = fmaxf(lag, dpp<ROR8>(lag)); lag = s16_max(lag);
      const u32 wait = (u32)__builtin_amdgcn_readfirstlane((int)fminf(fmaxf(lag * 0.5f, 0.f), 300.f)) + GO_C;
#pragma nounroll
      while ((u32)__builtin_amdgcn_s_memrealtime() - tb1 < wait) {}
    }
    if (lane < 32) *(volatile LAS u32*)(L + L_WT + (lane & 7) * 16 + (lane >> 3) * 4) = btag(wgt, (tlo >> ((lane >> 3) * 8)) & 255u);
  }
  {
    const int fbr = (blk >> 2) & 1, fcq = blk & 3, fb = blk >> 7, fe = (blk >> 3) & 15;
    const int pi = lane >> 1;
    int off = WS_H2 + ((pi & 7) + 8 * (16 * fbr + 4 * (pi >> 3) + fcq)) * H2_STR + fb * 512 + fe * 32 + (lane & 1) * 16;
    u32 ooff = (fb * 1024 + fbr * 512 + fcq * 128 + 32 * (fe >> 2) + 16 * (lane >> 5) + 4 * (fe & 3) + 2 * (lane & 1)) * 2;
    asm volatile("" : "+v"(off), "+v"(ooff));
    BAR();
    __builtin_amdgcn_s_sleep(H2_DELAY);
    u32x4 a = {1u, 1u, 1u, 1u};
    repoll1(a, off, rsv, tlo);
    float x[2];
#pragma unroll
    for (int e2 = 0; e2 < 2; ++e2) {
      auto t = __builtin_amdgcn_permlane32_swap(a[2 * e2], a[2 * e2 + 1], false, false);
      float v = __uint_as_float(t[0]) + __uint_as_float(t[1]);
      v = s16_sum(v); v += dpp<ROR8>(v); v += dpp<ROR4>(v); v += dpp<XOR2>(v);
      x[e2] = v;
    }
    if ((lane & 30) == 0) *(GAS u32*)((GAS unsigned char*)outp + ooff) = cvtpk(x[0], x[1]);
  }
}

extern "C" void kernel_launch(void* const* d_in, const int* in_sizes, int n_in, void* d_out, int out_size, void* d_ws, size_t ws_size, hipStream_t stream) {
  Params p;
  for (int br = 0; br < 2; ++br) {
    p.x[br] = (const u16*)d_in[br * 5 + 0]; p.k[br] = (const u16*)d_in[br * 5 + 1]; p.v[br] = (const u16*)d_in[br * 5 + 2];
    p.wq[br] = (const u16*)d_in[br * 5 + 3]; p.wo[br] = (const u16*)d_in[br * 5 + 4];
  }
  p.out = (u16*)d_out; p.ws = (unsigned char*)d_ws;
  fused_decode<<<256, 768, 0, stream>>>(p);
}
```
